```python
import torch

def _softmax(x, axis=-1):
    return torch.softmax(x, dim=axis)
def _gelu(x):
    return torch.nn.functional.gelu(x, approximate="tanh")
def _silu(x):
    return torch.nn.functional.silu(x)
def _sigmoid(x):
    return torch.sigmoid(x)
def _layernorm(x, axis=-1, eps=1e-5):
    m = x.mean(dim=axis, keepdim=True)
    v = x.var(dim=axis, keepdim=True, unbiased=False)
    return (x - m) / torch.sqrt(v + eps)
def _scatter_add(tgt, idx, src, axis=0):
    return tgt.clone().index_add_(axis, idx, src)
def _segment_sum(src, seg, n):
    out = torch.zeros((n,) + src.shape[1:], dtype=src.dtype)
    return out.index_add_(0, seg, src)
def _pad(x, pw):
    flat = tuple(v for pair in reversed(pw) for v in pair)
    return torch.nn.functional.pad(x, flat)

kernel_name = 'interleave_layers__spectral_gfnet__dense_activation_silu__prompt'

def setup_inputs():

    _w = (lambda a: (lambda t: t.to(torch.bfloat16) if t.is_floating_point() else t)(torch.as_tensor(a)))
    def _A_setup():
        B, S, D = 1, 2048, 640
        Df = D // 2 + 1
        return {
            "x": _w((lambda *s: torch.randn(*s, dtype=torch.bfloat16) * 0.05)(B, S, D)),
            "w_r": _w((lambda *s: torch.randn(*s, dtype=torch.bfloat16) * 0.4)(S, Df) * 0.02),
            "w_i": _w((lambda *s: torch.randn(*s, dtype=torch.bfloat16) * 0.4)(S, Df) * 0.02),
        }
    _A_in = _A_setup()

    def _B_setup():
        B, S, D, F = 1, 2048, 640, 1536
        return {
            "x": _w((lambda *s: torch.randn(*s, dtype=torch.bfloat16) * 0.05)(B, S, D)),
            "w1": _w((lambda *s: torch.randn(*s, dtype=torch.bfloat16) * 0.4)(D, F) * (D ** -0.5)),
            "w2": _w((lambda *s: torch.randn(*s, dtype=torch.bfloat16) * 0.1)(F, D) * (F ** -0.5)),
        }
    _B_in = _B_setup()

    return {
        "A_x": _A_in["x"],
        "A_w_r": _A_in["w_r"],
        "A_w_i": _A_in["w_i"],
        "B_x": _B_in["x"],
        "B_w1": _B_in["w1"],
        "B_w2": _B_in["w2"],
    }

def reference(A_x, A_w_r, A_w_i, B_x, B_w1, B_w2):
    def _A_ref(x, w_r, w_i):
        B, S, D = x.shape
        X = (lambda x, axes=(-2,-1): torch.fft.rfft2(x.float(), dim=axes))(x, axes=(-2, -1))
        W = w_r + 1j * w_i
        X = X * W
        y = (lambda x, s, axes=(-2,-1): torch.fft.irfft2(x, s=s, dim=axes).to(torch.bfloat16))(X, (S, D), axes=(-2, -1))
        return y

    def _B_ref(x, w1, w2):
        h = torch.einsum("bsd,df->bsf", x, w1)
        h = _silu(h)
        out = torch.einsum("bsf,fd->bsd", h, w2)
        return out

    x = A_x
    x = x + _A_ref(_layernorm(x, axis=-1), A_w_r, A_w_i)
    x = x + _B_ref(_layernorm(x, axis=-1), B_w1, B_w2)
    x = x + _A_ref(_layernorm(x, axis=-1), A_w_r, A_w_i)
    x = x + _B_ref(_layernorm(x, axis=-1), B_w1, B_w2)
    return x
```

```cpp
#include <hip/hip_runtime.h>
#include <hip/hip_bf16.h>
#include <stdint.h>
#include <utility>

typedef __bf16 bf16x8 __attribute__((ext_vector_type(8)));
typedef __bf16 bf16x4 __attribute__((ext_vector_type(4)));
typedef __bf16 bf16x2 __attribute__((ext_vector_type(2)));
typedef short s16x4 __attribute__((ext_vector_type(4)));
typedef short s16x8 __attribute__((ext_vector_type(8)));
typedef float f32x4 __attribute__((ext_vector_type(4)));
typedef float f32x2 __attribute__((ext_vector_type(2)));
typedef unsigned u32x4 __attribute__((ext_vector_type(4)));
typedef unsigned u32x2 __attribute__((ext_vector_type(2)));
typedef unsigned short u16;

#define DI __device__ __forceinline__
#define LAS __attribute__((address_space(3)))

constexpr int S_ = 2048, D_ = 640, FF_ = 1536;
constexpr float EPS_ = 1e-5f;

namespace tab {
constexpr double kPi = 3.14159265358979323846264338327950288;
constexpr double ccos(double x) {
  double x2 = x * x, term = 1.0, sum = 1.0;
  for (int n = 1; n <= 24; ++n) { term *= -x2 / (double)((2 * n - 1) * (2 * n)); sum += term; }
  return sum;
}
constexpr uint16_t tobf(double v) {
  float f = (float)v;
  uint32_t u = __builtin_bit_cast(uint32_t, f);
  u += 0x7fffu + ((u >> 16) & 1u);
  return (uint16_t)(u >> 16);
}
struct Cos { uint16_t c[640]; };
constexpr Cos mkcos() {
  Cos t{};
  const double sc = 0.05590169943749474241;
  for (int j = 0; j < 640; ++j) {
    int jj = j <= 320 ? j : 640 - j;
    double v = (jj == 160) ? 0.0 : ccos(2.0 * kPi * jj / 640.0);
    t.c[j] = tobf(sc * v);
  }
  return t;
}
constexpr Cos kCos = mkcos();
constexpr uint16_t kR = tobf(0.03952847075210474165);
constexpr uint16_t fval(int d, int kk) {
  int c = kk >> 1, p = kk & 1;
  if (c == 0) return (p == 0 && (d & 1)) ? (uint16_t)(kR | 0x8000u) : kR;
  return kCos.c[(d * c + 160 * p) % 640];
}
struct Chunk { uint64_t v[20 * 64 * 2]; };
constexpr Chunk mk(int n16, bool transposed) {
  Chunk ch{};
  for (int k32 = 0; k32 < 20; ++k32)
    for (int l = 0; l < 64; ++l) {
      int n = n16 * 16 + (l & 15), k0 = k32 * 32 + (l >> 4) * 8;
      for (int h = 0; h < 2; ++h) {
        uint64_t w = 0;
        for (int j = 0; j < 4; ++j) {
          int k = k0 + h * 4 + j;
          w |= (uint64_t)(transposed ? fval(n, k) : fval(k, n)) << (16 * j);
        }
        ch.v[(k32 * 64 + l) * 2 + h] = w;
      }
    }
  return ch;
}
template <int I> constexpr Chunk cFB = mk(I, false);
template <int I> constexpr Chunk cFT = mk(I, true);
struct Tab { Chunk c[40]; };
template <int... I> constexpr Tab aggB(std::integer_sequence<int, I...>) { return Tab{{cFB<I>...}}; }
template <int... I> constexpr Tab aggT(std::integer_sequence<int, I...>) { return Tab{{cFT<I>...}}; }
}
#if defined(__HIP_DEVICE_COMPILE__)
static __device__ const __attribute__((aligned(1024))) tab::Tab g_FB = tab::aggB(std::make_integer_sequence<int, 40>{});
static __device__ const __attribute__((aligned(1024))) tab::Tab g_FT = tab::aggT(std::make_integer_sequence<int, 40>{});
#else
static __device__ const __attribute__((aligned(1024))) tab::Tab g_FB = {};
static __device__ const __attribute__((aligned(1024))) tab::Tab g_FT = {};
#endif

template <int... I, class F> DI void sfor_impl(std::integer_sequence<int, I...>, F&& f) {
  (f(std::integral_constant<int, I>{}), ...);
}
template <int N, class F> DI void sfor(F&& f) { sfor_impl(std::make_integer_sequence<int, N>{}, f); }

DI f32x4 mfma16(bf16x8 a, bf16x8 b, f32x4 c) { return __builtin_amdgcn_mfma_f32_16x16x32_bf16(a, b, c, 0, 0, 0); }

template <int OFF> DI void glds16(const void* sbase, unsigned voff, unsigned lds_dst) {
  unsigned keep;
  asm volatile("s_mov_b32 %0, m0\n\ts_mov_b32 m0, %3\n\ts_nop 0\n\tglobal_load_lds_dwordx4 %1, %2 offset:%4\n\ts_mov_b32 m0, %0"
               : "=&s"(keep) : "v"(voff), "s"(sbase), "s"(lds_dst), "n"(OFF) : "memory");
}
template <int N> DI void wait_bar() {
  asm volatile("s_waitcnt vmcnt(%0) lgkmcnt(0)\n\ts_barrier" ::"n"(N) : "memory");
}
DI bf16x8 lds128(unsigned addr) { return *(const LAS bf16x8*)(uintptr_t)addr; }
DI bf16x8 lds_tr(unsigned a0, unsigned a1) {
  s16x4 lo = __builtin_amdgcn_ds_read_tr16_b64_v4i16((LAS s16x4*)(uintptr_t)a0);
  s16x4 hi = __builtin_amdgcn_ds_read_tr16_b64_v4i16((LAS s16x4*)(uintptr_t)a1);
  s16x8 v = __builtin_shufflevector(lo, hi, 0, 1, 2, 3, 4, 5, 6, 7);
  return __builtin_bit_cast(bf16x8, v);
}
DI unsigned pack2(float a, float b) {
  f32x2 f = {a, b};
  return __builtin_bit_cast(unsigned, __builtin_convertvector(f, bf16x2));
}
DI u32x2 pack4(f32x4 v) { return u32x2{pack2(v[0], v[1]), pack2(v[2], v[3])}; }
DI bf16x8 pack8(f32x4 a, f32x4 b) {
  u32x4 u = {pack2(a[0], a[1]), pack2(a[2], a[3]), pack2(b[0], b[1]), pack2(b[2], b[3])};
  return __builtin_bit_cast(bf16x8, u);
}
DI float bflo(unsigned u) { return __builtin_bit_cast(float, u << 16); }
DI float bfhi(unsigned u) { return __builtin_bit_cast(float, u & 0xffff0000u); }
DI f32x4 unpack4(u32x2 u) { return f32x4{bflo(u[0]), bfhi(u[0]), bflo(u[1]), bfhi(u[1])}; }
DI bf16x8 neg8(bf16x8 v) {
  u32x4 u = __builtin_bit_cast(u32x4, v);
  u ^= 0x80008000u;
  return __builtin_bit_cast(bf16x8, u);
}
DI int row_of(int m) { return 32 * (m & 63) + (m >> 6); }
DI int uni(int v) { return __builtin_amdgcn_readfirstlane(v); }

constexpr int G_STAGE = 18432, G_QOFF = 8192;

struct PSrc {
  const char* base;
  unsigned v0, v1;
  unsigned step;
};

template <int PKIND> struct PRead {
  unsigned a0, a1;
};

template <int NS, int T1, int PK1, int T2, int PK2, bool P_IS_A>
DI void gemm64x80(f32x4 (&acc)[5], unsigned lds0, int w, int lane,
                  const char* p1base, unsigned p1v0, unsigned p1v1, unsigned p1step, const char* q1base, unsigned q1stride,
                  const char* p2base, unsigned p2v0, unsigned p2v1, unsigned p2step, const char* q2base, unsigned q2stride) {
  constexpr int T = T1 + T2;
  const int g = lane >> 4, i = lane & 15;
  const unsigned lane16 = lane * 16;
  const unsigned rdF = w * 2048 + lane16;
  const unsigned rdR0 = w * 2048 + i * 128 + ((unsigned)((0 + g) ^ (i & 7)) << 4);
  const unsigned rdR1 = w * 2048 + i * 128 + ((unsigned)((4 + g) ^ (i & 7)) << 4);
  const int q4 = i >> 2, p4 = i & 3;
  const unsigned keyT = ((q4 >> 1) & 1) | ((g & 1) << 1);
  const unsigned rdT = (8 * g + q4) * 128 + (((unsigned)(2 * w + (p4 >> 1)) ^ (keyT << 1)) << 4) + 8 * (p4 & 1);
  const unsigned rdQ = G_QOFF + lane16;
  const bool lo = w < 2;

  auto issue = [&](auto tc) {
    constexpr int t = decltype(tc)::value;
    const unsigned slot = lds0 + (t % NS) * G_STAGE;
    constexpr bool ph1 = t < T1;
    constexpr int tt = ph1 ? t : t - T1;
    constexpr int PK = ph1 ? PK1 : PK2;
    const char* pb = (ph1 ? p1base : p2base) + (size_t)tt * (ph1 ? p1step : p2step);
    const char* qb = (ph1 ? q1base : q2base) + (size_t)tt * 2048;
    const unsigned qs = ph1 ? q1stride : q2stride;
    if constexpr (PK == 1) {
      glds16<0>(pb, ph1 ? p1v0 : p2v0, slot + w * 2048);
      glds16<1024>(pb, ph1 ? p1v0 : p2v0, slot + w * 2048);
    } else {
      glds16<0>(pb, ph1 ? p1v0 : p2v0, slot + w * 2048);
      glds16<0>(pb, ph1 ? p1v1 : p2v1, slot + w * 2048 + 1024);
    }
    glds16<0>(qb + (size_t)w * qs, lane16, slot + G_QOFF + w * 2048);
    glds16<1024>(qb + (size_t)w * qs, lane16, slot + G_QOFF + w * 2048);
    if (lo) glds16<0>(qb + (size_t)4 * qs + w * 1024, lane16, slot + G_QOFF + 8192 + w * 1024);
  };

  sfor<(NS - 1 < T ? NS - 1 : T)>(issue);
  sfor<T>([&](auto tc) {
    constexpr int t = decltype(tc)::value;
    constexpr int ahead = (t + NS - 2 < T - 1 ? t + NS - 2 : T - 1) - t;
    if (lo) wait_bar<5 * ahead>(); else wait_bar<4 * ahead>();
    if constexpr (t + NS - 1 < T) issue(std::integral_constant<int, t + NS - 1>{});
    const unsigned slot = lds0 + (t % NS) * G_STAGE;
    constexpr int PK = t < T1 ? PK1 : PK2;
#pragma unroll
    for (int ks = 0; ks < 2; ++ks) {
      bf16x8 pf;
      if constexpr (PK == 0) pf = lds128(slot + (ks ? rdR1 : rdR0));
      else if constexpr (PK == 1) pf = lds128(slot + rdF + ks * 1024);
      else pf = lds_tr(slot + rdT + ks * 4096, slot + rdT + ks * 4096 + 512);
#pragma unroll
      for (int q = 0; q < 5; ++q) {
        bf16x8 qf = lds128(slot + rdQ + q * 2048 + ks * 1024);
        acc[q] = P_IS_A ? mfma16(pf, qf, acc[q]) : mfma16(qf, pf, acc[q]);
      }
    }
  });
}

DI void poff_rows(int w, int lane, int row0, bool permuted, unsigned& v0, unsigned& v1) {
  const int r = lane >> 3, ch = (lane & 7) ^ (r & 7);
  int ra = row0 + 16 * w + r, rb = ra + 8;
  if (permuted) { ra = row_of(ra); rb = row_of(rb); }
  v0 = ra * 1280 + ch * 16;
  v1 = rb * 1280 + ch * 16;
}
DI void poff_kmajor(int w, int lane, unsigned rowbytes, unsigned& v0, unsigned& v1) {
  const int r = lane >> 3, chp = lane & 7;
  const int row_a = 16 * w + r, row_b = row_a + 8;
  const unsigned ka = ((row_a >> 1) & 1) | (((row_a >> 3) & 1) << 1), kb = ((row_b >> 1) & 1) | (((row_b >> 3) & 1) << 1);
  v0 = row_a * rowbytes + ((chp ^ (ka << 1)) << 4);
  v1 = row_b * rowbytes + ((chp ^ (kb << 1)) << 4);
}

constexpr int K1_NS = 4;
__global__ __launch_bounds__(256) void k1_rfft(const u16* __restrict__ x, const u16* __restrict__ wr, const u16* __restrict__ wi,
                                              u16* __restrict__ X0T, float* __restrict__ rstd0, unsigned* __restrict__ WT) {
  extern __shared__ __attribute__((aligned(16))) char smem[];
  const unsigned lds0 = (unsigned)(uintptr_t)(LAS char*)smem;
  const int tid = threadIdx.x, lane = tid & 63, w = uni(tid >> 6);
  const int b = blockIdx.x;
  if (b >= 256) {
    const int bb = b - 256, kt = bb % 10, ot = bb / 10;
    unsigned* tile = (unsigned*)smem;
    const int kc = tid & 31, k = kt * 32 + kc + 1;
#pragma unroll
    for (int it = 0; it < 16; ++it) {
      const int ol = (tid >> 5) + 8 * it, o = ot * 128 + ol;
      const int e = o & 7, ln = (o >> 3) & 63, ww = o >> 9;
      const int t = 16 * ww + (ln & 15) + 64 * (16 * (e >> 2) + 4 * (ln >> 4) + (e & 3));
      tile[kc * 129 + ol] = (unsigned)wr[t * 321 + k] | ((unsigned)wi[t * 321 + k] << 16);
    }
    __syncthreads();
#pragma unroll
    for (int it = 0; it < 16; ++it) {
      const int idx = tid + 256 * it, kc2 = idx >> 7, ol = idx & 127;
      const int c = (kt * 32 + kc2 + 1) % 320;
      WT[c * 2048 + ot * 128 + ol] = tile[kc2 * 129 + ol];
    }
    return;
  }
  const int xcd = b & 7, j = b >> 3;
  const int mt = (xcd >> 1) * 8 + (j & 7), nt = (xcd & 1) * 4 + (j >> 3);
  unsigned v0, v1;
  poff_rows(w, lane, mt * 64, true, v0, v1);
  f32x4 acc[5] = {};
  u32x4 sa[2]; unsigned sb[2];
#pragma unroll
  for (int rr = 0; rr < 2; ++rr) {
    const u16* xr = x + (size_t)row_of(8 * b + 2 * w + rr) * D_;
    sa[rr] = *(const u32x4*)(xr + lane * 8);
    sb[rr] = *(const unsigned*)(xr + 512 + lane * 2);
  }
  gemm64x80<K1_NS, 10, 0, 0, 0, true>(acc, lds0, w, lane, (const char*)x, v0, v1, 128, (const char*)&g_FB + (size_t)nt * 5 * 20480, 20480,
                                      nullptr, 0, 0, 0, nullptr, 0);
#pragma unroll
  for (int rr = 0; rr < 2; ++rr) {
    float v[10] = {bflo(sa[rr][0]), bfhi(sa[rr][0]), bflo(sa[rr][1]), bfhi(sa[rr][1]), bflo(sa[rr][2]), bfhi(sa[rr][2]),
                   bflo(sa[rr][3]), bfhi(sa[rr][3]), bflo(sb[rr]), bfhi(sb[rr])};
    float s = 0;
#pragma unroll
    for (int e = 0; e < 10; ++e) s += v[e];
#pragma unroll
    for (int o = 32; o; o >>= 1) s += __shfl_xor(s, o);
    const float mean = s * (1.f / D_);
    float q = 0;
#pragma unroll
    for (int e = 0; e < 10; ++e) { float d = v[e] - mean; q += d * d; }
#pragma unroll
    for (int o = 32; o; o >>= 1) q += __shfl_xor(q, o);
    if (lane == 0) rstd0[8 * b + 2 * w + rr] = rsqrtf(q * (1.f / D_) + EPS_);
  }
  const int g = lane >> 4, i = lane & 15;
#pragma unroll
  for (int q = 0; q < 5; ++q) {
    const int kk = nt * 80 + q * 16 + i;
    *(u32x2*)(X0T + (size_t)kk * S_ + mt * 64 + w * 16 + g * 4) = pack4(acc[q]);
  }
}

template <bool SECOND>
DI void column_block(int c, char* smem, const u16* __restrict__ Xin, const float* __restrict__ stat, const unsigned* __restrict__ WT,
                     u16* __restrict__ Xout, const u16* __restrict__ X0T, u16* __restrict__ Dout) {
  const unsigned lds0 = (unsigned)(uintptr_t)(LAS char*)smem;
  const int tid = threadIdx.x, lane = tid & 63, w = uni(tid >> 6), g = lane >> 4, i = lane & 15;
  constexpr int CH = 528, PL = 8 * CH;
  const u16* xre = Xin + (size_t)(2 * c) * S_;
  const u16* xim = xre + S_;
  const u32x4 re8 = *(const u32x4*)(xre + tid * 8);
  u32x4 im8 = *(const u32x4*)(xim + tid * 8);
  float rs[8];
  if constexpr (!SECOND) {
    f32x4 a = *(const f32x4*)(stat + tid * 8), b = *(const f32x4*)(stat + tid * 8 + 4);
#pragma unroll
    for (int e = 0; e < 4; ++e) { rs[e] = a[e]; rs[4 + e] = b[e]; }
  } else {
    f32x4 a = {}, b = {};
#pragma unroll
    for (int p = 0; p < 8; ++p) {
      a += *(const f32x4*)(stat + p * S_ + tid * 8);
      b += *(const f32x4*)(stat + p * S_ + tid * 8 + 4);
    }
#pragma unroll
    for (int e = 0; e < 4; ++e) { rs[e] = rsqrtf(a[e] * (1.f / D_) + EPS_); rs[4 + e] = rsqrtf(b[e] * (1.f / D_) + EPS_); }
  }
  const u32x4 wa = *(const u32x4*)(WT + (size_t)c * 2048 + tid * 8);
  const u32x4 wb = *(const u32x4*)(WT + (size_t)c * 2048 + tid * 8 + 4);
  u32x2 rr[2], ri[2], zr0[2], zi0[2];
#pragma unroll
  for (int nt = 0; nt < 2; ++nt) {
    const int m = (16 * nt + i) * 64 + 16 * w + 4 * g;
    rr[nt] = *(const u32x2*)(xre + m);
    ri[nt] = *(const u32x2*)(xim + m);
    if constexpr (SECOND) {
      zr0[nt] = *(const u32x2*)(X0T + (size_t)(2 * c) * S_ + m);
      zi0[nt] = *(const u32x2*)(X0T + (size_t)(2 * c + 1) * S_ + m);
    }
  }
  bf16x8 f1r[2], f1i[2], f1n[2], f2r[2], f2i[2], f2n[2];
  const int k1c = 16 * w + i;
#pragma unroll
  for (int ks = 0; ks < 2; ++ks) {
    f32x4 cr[2], ci[2];
#pragma unroll
    for (int j = 0; j < 8; ++j) {
      const float a = (float)(((32 * ks + 8 * g + j) * k1c) & 63) * (1.f / 64.f);
      cr[j >> 2][j & 3] = 0.125f * __builtin_amdgcn_cosf(a);
      ci[j >> 2][j & 3] = -0.125f * __builtin_amdgcn_sinf(a);
    }
    f1r[ks] = pack8(cr[0], cr[1]);
    f1i[ks] = pack8(ci[0], ci[1]);
    f1n[ks] = neg8(f1i[ks]);
  }
#pragma unroll
  for (int mt = 0; mt < 2; ++mt) {
    f32x4 cr[2], ci[2];
#pragma unroll
    for (int j = 0; j < 8; ++j) {
      const float a = (float)(((16 * mt + i) * (16 * (j >> 2) + 4 * g + (j & 3))) & 31) * (1.f / 32.f);
      cr[j >> 2][j & 3] = 0.17677669529663688f * __builtin_amdgcn_cosf(a);
      ci[j >> 2][j & 3] = -0.17677669529663688f * __builtin_amdgcn_sinf(a);
    }
    f2r[mt] = pack8(cr[0], cr[1]);
    f2i[mt] = pack8(ci[0], ci[1]);
    f2n[mt] = neg8(f2i[mt]);
  }
  f32x4 twc[2], tws[2], tvc[2], tvs[2];
#pragma unroll
  for (int mt = 0; mt < 2; ++mt)
#pragma unroll
    for (int r = 0; r < 4; ++r) {
      const float a = (float)((16 * mt + 4 * g + r) * k1c) * (1.f / 2048.f);
      twc[mt][r] = __builtin_amdgcn_cosf(a);
      tws[mt][r] = __builtin_amdgcn_sinf(a);
      const float a2 = (float)((16 * mt + i) * (16 * w + 4 * g + r)) * (1.f / 2048.f);
      tvc[mt][r] = __builtin_amdgcn_cosf(a2);
      tvs[mt][r] = __builtin_amdgcn_sinf(a2);
    }
  {
    if (c == 0) im8 = u32x4{0, 0, 0, 0};
    u32x4 a, b;
#pragma unroll
    for (int e = 0; e < 4; ++e) {
      a[e] = pack2(bflo(re8[e]) * rs[2 * e], bfhi(re8[e]) * rs[2 * e + 1]);
      b[e] = pack2(bflo(im8[e]) * rs[2 * e], bfhi(im8[e]) * rs[2 * e + 1]);
    }
    const unsigned ad = lds0 + (tid & 7) * CH + (tid >> 3) * 16;
    *(LAS u32x4*)(uintptr_t)ad = a;
    *(LAS u32x4*)(uintptr_t)(ad + PL) = b;
  }
  __syncthreads();
  const unsigned rd = lds0 + g * CH + i * 16;
  f32x4 ar[2] = {}, ai[2] = {};
#pragma unroll
  for (int mt = 0; mt < 2; ++mt)
#pragma unroll
    for (int ks = 0; ks < 2; ++ks) {
      const bf16x8 ur = lds128(rd + ks * 4 * CH + mt * 256), ui = lds128(rd + PL + ks * 4 * CH + mt * 256);
      ar[mt] = mfma16(ur, f1r[ks], ar[mt]);
      ai[mt] = mfma16(ur, f1i[ks], ai[mt]);
      ar[mt] = mfma16(ui, f1n[ks], ar[mt]);
      ai[mt] = mfma16(ui, f1r[ks], ai[mt]);
    }
  f32x4 br[2], bi[2];
#pragma unroll
  for (int mt = 0; mt < 2; ++mt) {
    br[mt] = ar[mt] * twc[mt] + ai[mt] * tws[mt];
    bi[mt] = ai[mt] * twc[mt] - ar[mt] * tws[mt];
  }
  bf16x8 pr = pack8(br[0], br[1]), pi = pack8(bi[0], bi[1]);
  f32x4 yr[2] = {}, yi[2] = {};
#pragma unroll
  for (int mt = 0; mt < 2; ++mt) {
    yr[mt] = mfma16(f2r[mt], pr, yr[mt]);
    yi[mt] = mfma16(f2i[mt], pr, yi[mt]);
    yr[mt] = mfma16(f2n[mt], pi, yr[mt]);
    yi[mt] = mfma16(f2r[mt], pi, yi[mt]);
  }
#pragma unroll
  for (int mt = 0; mt < 2; ++mt) {
    const u32x4 ww = mt ? wb : wa;
#pragma unroll
    for (int r = 0; r < 4; ++r) {
      const float wr_ = bflo(ww[r]), wi_ = bfhi(ww[r]);
      br[mt][r] = yr[mt][r] * wr_ - yi[mt][r] * wi_;
      bi[mt][r] = yr[mt][r] * wi_ + yi[mt][r] * wr_;
    }
  }
  pr = pack8(br[0], br[1]);
  pi = pack8(bi[0], bi[1]);
  f32x4 zr[2] = {}, zi[2] = {};
#pragma unroll
  for (int nt = 0; nt < 2; ++nt) {
    zr[nt] = mfma16(pr, f2r[nt], zr[nt]);
    zi[nt] = mfma16(pi, f2r[nt], zi[nt]);
    zr[nt] = mfma16(pi, f2i[nt], zr[nt]);
    zi[nt] = mfma16(pr, f2n[nt], zi[nt]);
  }
  {
    const unsigned ad = lds0 + 2 * PL + (2 * w + (g >> 1)) * CH + i * 16 + (g & 1) * 8;
#pragma unroll
    for (int nt = 0; nt < 2; ++nt) {
      const f32x4 a = zr[nt] * tvc[nt] - zi[nt] * tvs[nt];
      const f32x4 b = zi[nt] * tvc[nt] + zr[nt] * tvs[nt];
      *(LAS u32x2*)(uintptr_t)(ad + nt * 256) = pack4(a);
      *(LAS u32x2*)(uintptr_t)(ad + PL + nt * 256) = pack4(b);
    }
  }
  __syncthreads();
  f32x4 vr[2] = {}, vi[2] = {};
#pragma unroll
  for (int nt = 0; nt < 2; ++nt)
#pragma unroll
    for (int ks = 0; ks < 2; ++ks) {
      const bf16x8 qr = lds128(rd + 2 * PL + ks * 4 * CH + nt * 256), qi = lds128(rd + 3 * PL + ks * 4 * CH + nt * 256);
      vr[nt] = mfma16(f1r[ks], qr, vr[nt]);
      vi[nt] = mfma16(f1r[ks], qi, vi[nt]);
      vr[nt] = mfma16(f1i[ks], qi, vr[nt]);
      vi[nt] = mfma16(f1n[ks], qr, vi[nt]);
    }
  u16* ore = Xout + (size_t)(2 * c) * S_;
#pragma unroll
  for (int nt = 0; nt < 2; ++nt) {
    const int m = (16 * nt + i) * 64 + 16 * w + 4 * g;
    const f32x4 a = unpack4(rr[nt]) + vr[nt];
    f32x4 b = unpack4(ri[nt]);
    if (c != 0) b += vi[nt];
    *(u32x2*)(ore + m) = pack4(a);
    *(u32x2*)(ore + S_ + m) = pack4(b);
    if constexpr (SECOND) {
      *(u32x2*)(Dout + (size_t)(2 * c) * S_ + m) = pack4(a - unpack4(zr0[nt]));
      *(u32x2*)(Dout + (size_t)(2 * c + 1) * S_ + m) = pack4(b - unpack4(zi0[nt]));
    }
  }
}

constexpr int K0_NS = 2;
__global__ __launch_bounds__(256) void k2_cols(const u16* __restrict__ X0T, const float* __restrict__ rstd0, const unsigned* __restrict__ WT,
                                              u16* __restrict__ X1T, const u16* __restrict__ w1, const u16* __restrict__ w2,
                                              u16* __restrict__ W1F, u16* __restrict__ W2F) {
  extern __shared__ __attribute__((aligned(16))) char smem[];
  const int b = blockIdx.x;
  if (b < 320) {
    column_block<false>(b, smem, X0T, rstd0, WT, X1T, nullptr, nullptr);
    return;
  }
  const unsigned lds0 = (unsigned)(uintptr_t)(LAS char*)smem;
  const int tid = threadIdx.x, lane = tid & 63, w = uni(tid >> 6), g = lane >> 4, i = lane & 15;
  f32x4 acc[5] = {};
  unsigned v0, v1;
  if (b < 512) {
    const int bb = b - 320, nt = bb & 7, ft = bb >> 3;
    poff_kmajor(w, lane, FF_ * 2, v0, v1);
    gemm64x80<K0_NS, 10, 2, 0, 0, false>(acc, lds0, w, lane, (const char*)w1 + ft * 128, v0, v1, 64 * FF_ * 2,
                                         (const char*)&g_FB + (size_t)nt * 5 * 20480, 20480, nullptr, 0, 0, 0, nullptr, 0);
    const int f16 = ft * 4 + w;
#pragma unroll
    for (int q = 0; q < 5; ++q) {
      const int kk16 = nt * 5 + q, k32 = kk16 >> 1, hh = kk16 & 1;
      f32x4 v = acc[q];
      if (kk16 == 0 && g == 0) v[1] = 0.f;
      *(u32x2*)((char*)W1F + ((size_t)(f16 * 20 + k32) * 64 + (2 * hh + (g >> 1)) * 16 + i) * 16 + (g & 1) * 8) = pack4(v);
    }
  } else {
    const int bb = b - 512, nt = bb & 7, ft = bb >> 3;
    poff_rows(w, lane, ft * 64, false, v0, v1);
    gemm64x80<K0_NS, 10, 0, 0, 0, true>(acc, lds0, w, lane, (const char*)w2, v0, v1, 128,
                                        (const char*)&g_FB + (size_t)nt * 5 * 20480, 20480, nullptr, 0, 0, 0, nullptr, 0);
    const int f16 = ft * 4 + w, k32 = f16 >> 1, hh = f16 & 1;
#pragma unroll
    for (int q = 0; q < 5; ++q) {
      const int kk16 = nt * 5 + q;
      *(u32x2*)((char*)W2F + ((size_t)(kk16 * 48 + k32) * 64 + lane) * 16 + hh * 8) = pack4(acc[q]);
    }
  }
}

__global__ __launch_bounds__(256) void k5_cols(const u16* __restrict__ X2T, const float* __restrict__ part, const unsigned* __restrict__ WT,
                                              u16* __restrict__ X3T, const u16* __restrict__ X0T, u16* __restrict__ D3T,
                                              const u16* __restrict__ w2, u16* __restrict__ W2P) {
  extern __shared__ __attribute__((aligned(16))) char smem[];
  const int b = blockIdx.x;
  if (b < 320) {
    column_block<true>(b, smem, X2T, part, WT, X3T, X0T, D3T);
    return;
  }
  const int fr = (b - 320) * 4 + (threadIdx.x >> 6), lane = threadIdx.x & 63, g = lane >> 4, i = lane & 15;
  const int d16 = fr % 40, f32i = fr / 40;
  const u16* src = w2 + (size_t)(f32i * 32 + 4 * g) * D_ + d16 * 16 + i;
  u32x4 o;
#pragma unroll
  for (int h = 0; h < 2; ++h) {
    o[2 * h] = (unsigned)src[(16 * h + 0) * D_] | ((unsigned)src[(16 * h + 1) * D_] << 16);
    o[2 * h + 1] = (unsigned)src[(16 * h + 2) * D_] | ((unsigned)src[(16 * h + 3) * D_] << 16);
  }
  *(u32x4*)((char*)W2P + ((size_t)(d16 * 48 + f32i) * 64 + lane) * 16) = o;
}

constexpr int G1_NS = 5, G1_STAGE = 28672, G1_WOFF = 16384;
__global__ __launch_bounds__(512) void k3_gemm1(const u16* __restrict__ XT, const u16* __restrict__ W1F, u16* __restrict__ H) {
  extern __shared__ __attribute__((aligned(16))) char smem[];
  const unsigned lds0 = (unsigned)(uintptr_t)(LAS char*)smem;
  const int tid = threadIdx.x, lane = tid & 63, w = uni(tid >> 6), g = lane >> 4, i = lane & 15;
  const int wm = w & 3, wf = w >> 2;
  const int b = blockIdx.x, xcd = b & 7, j = b >> 3;
  const int mt = (xcd >> 1) * 4 + (j & 3), ft = (xcd & 1) * 8 + (j >> 2);
  constexpr int T = 10, NS = G1_NS;
  unsigned xv0, xv1;
  {
    const int r = lane >> 4, chp = lane & 15;
    const int ra = 8 * w + r, rb = ra + 4;
    const unsigned ka = (ra & 3) | (((ra >> 3) & 1) << 2), kb = (rb & 3) | (((rb >> 3) & 1) << 2);
    xv0 = ra * (S_ * 2) + ((chp ^ (ka << 1)) << 4);
    xv1 = rb * (S_ * 2) + ((chp ^ (kb << 1)) << 4);
  }
  const char* xbase = (const char*)XT + mt * 256;
  const char* wbase = (const char*)W1F + (size_t)(ft * 6 + (w < 6 ? w : 0)) * 20480;
  const unsigned lane16 = lane * 16;
  const bool lo = w < 6;
  const int q4 = i >> 2, p4 = i & 3;
  const unsigned key = q4 | ((g & 1) << 2);
  unsigned rdX[2];
#pragma unroll
  for (int c = 0; c < 2; ++c)
    rdX[c] = (8 * g + q4) * 256 + (((unsigned)(2 * (2 * wm + c) + (p4 >> 1)) ^ (key << 1)) << 4) + 8 * (p4 & 1);
  const unsigned rdW = G1_WOFF + wf * 3 * 2048 + lane16;

  auto issue = [&](auto tc) {
    constexpr int t = decltype(tc)::value;
    const unsigned slot = lds0 + (t % NS) * G1_STAGE;
    glds16<0>(xbase + (size_t)t * 64 * S_ * 2, xv0, slot + w * 2048);
    glds16<0>(xbase + (size_t)t * 64 * S_ * 2, xv1, slot + w * 2048 + 1024);
    if (lo) {
      glds16<0>(wbase + t * 2048, lane16, slot + G1_WOFF + w * 2048);
      glds16<1024>(wbase + t * 2048, lane16, slot + G1_WOFF + w * 2048);
    }
  };
  f32x4 acc[3][2] = {};
  float ss[2][2] = {};
  sfor<NS - 1>(issue);
  sfor<T>([&](auto tc) {
    constexpr int t = decltype(tc)::value;
    constexpr int ahead = (t + NS - 2 < T - 1 ? t + NS - 2 : T - 1) - t;
    if (lo) wait_bar<4 * ahead>(); else wait_bar<2 * ahead>();
    if constexpr (t + NS - 1 < T) issue(std::integral_constant<int, t + NS - 1>{});
    const unsigned slot = lds0 + (t % NS) * G1_STAGE;
#pragma unroll
    for (int ks = 0; ks < 2; ++ks) {
      bf16x8 xf[2], wfr[3];
#pragma unroll
      for (int c = 0; c < 2; ++c) xf[c] = lds_tr(slot + rdX[c] + ks * 8192, slot + rdX[c] + ks * 8192 + 1024);
#pragma unroll
      for (int q = 0; q < 3; ++q) wfr[q] = lds128(slot + rdW + q * 2048 + ks * 1024);
#pragma unroll
      for (int q = 0; q < 3; ++q)
#pragma unroll
        for (int c = 0; c < 2; ++c) acc[q][c] = mfma16(wfr[q], xf[c], acc[q][c]);
#pragma unroll
      for (int c = 0; c < 2; ++c) {
        u32x4 u = __builtin_bit_cast(u32x4, xf[c]);
        if (t == 0 && ks == 0 && g == 0) u[0] &= 0xffffu;
#pragma unroll
        for (int e = 0; e < 4; ++e) {
          const unsigned ue = u[e];
          const bf16x2 p = __builtin_bit_cast(bf16x2, ue);
          ss[c][e & 1] = __builtin_amdgcn_fdot2_f32_bf16(p, p, ss[c][e & 1], false);
        }
        asm volatile("" : "+v"(ss[c][0]), "+v"(ss[c][1]));
      }
    }
  });
#pragma unroll
  for (int c = 0; c < 2; ++c) {
    float s = ss[c][0] + ss[c][1];
    s += __shfl_xor(s, 16);
    s += __shfl_xor(s, 32);
    const float rstd = rsqrtf(s * (1.f / D_) + EPS_);
    u32x2 hv[3];
#pragma unroll
    for (int q = 0; q < 3; ++q) {
      f32x4 v = acc[q][c] * rstd, o;
#pragma unroll
      for (int r = 0; r < 4; ++r) o[r] = v[r] * __builtin_amdgcn_rcpf(1.f + __builtin_amdgcn_exp2f(v[r] * -1.4426950408889634f));
      hv[q] = pack4(o);
    }
    char* hb = (char*)H + ((size_t)((mt * 8 + wm * 2 + c) * 48 + ft * 3) * 64 + lane) * 16;
    if (wf == 0) {
      *(u32x4*)(hb) = u32x4{hv[0][0], hv[0][1], hv[1][0], hv[1][1]};
      *(u32x2*)(hb + 1024) = hv[2];
    } else {
      *(u32x2*)(hb + 1024 + 8) = hv[0];
      *(u32x4*)(hb + 2048) = u32x4{hv[1][0], hv[1][1], hv[2][0], hv[2][1]};
    }
  }
}

constexpr int K4_NS = 8;
__global__ __launch_bounds__(256) void k4_gemm2(const u16* __restrict__ H, const u16* __restrict__ W2F, const u16* __restrict__ X1T,
                                               u16* __restrict__ X2T, float* __restrict__ part) {
  extern __shared__ __attribute__((aligned(16))) char smem[];
  const unsigned lds0 = (unsigned)(uintptr_t)(LAS char*)smem;
  const int tid = threadIdx.x, lane = tid & 63, w = uni(tid >> 6), g = lane >> 4, i = lane & 15;
  const int b = blockIdx.x, xcd = b & 7, j = b >> 3;
  const int mt = (xcd >> 1) * 8 + (j & 7), nt = (xcd & 1) * 4 + (j >> 3);
  f32x4 acc[5] = {};
  gemm64x80<K4_NS, 24, 1, 0, 0, true>(acc, lds0, w, lane, (const char*)H + (size_t)(mt * 4 + w) * 49152, lane * 16, 0, 2048,
                                      (const char*)W2F + (size_t)nt * 5 * 49152, 49152, nullptr, 0, 0, 0, nullptr, 0);
  f32x4 sq = {};
#pragma unroll
  for (int q = 0; q < 5; ++q) {
    const int kk = nt * 80 + q * 16 + i;
    const size_t o = (size_t)kk * S_ + mt * 64 + w * 16 + g * 4;
    const f32x4 v = unpack4(*(const u32x2*)(X1T + o)) + acc[q];
    *(u32x2*)(X2T + o) = pack4(v);
    if (kk != 1) sq += v * v;
  }
#pragma unroll
  for (int o = 8; o; o >>= 1)
#pragma unroll
    for (int r = 0; r < 4; ++r) sq[r] += __shfl_xor(sq[r], o);
  if (i == 0) *(f32x4*)(part + nt * S_ + mt * 64 + w * 16 + g * 4) = sq;
}

__global__ __launch_bounds__(256) void k7_out(const u16* __restrict__ H, const u16* __restrict__ W2P, const u16* __restrict__ D3T,
                                             const u16* __restrict__ x, u16* __restrict__ out) {
  extern __shared__ __attribute__((aligned(16))) char smem[];
  const unsigned lds0 = (unsigned)(uintptr_t)(LAS char*)smem;
  const int tid = threadIdx.x, lane = tid & 63, w = uni(tid >> 6), g = lane >> 4, i = lane & 15;
  const int b = blockIdx.x, xcd = b & 7, j = b >> 3;
  const int mt = (xcd >> 1) * 8 + (j & 7), nt = (xcd & 1) * 4 + (j >> 3);
  f32x4 acc[5] = {};
  unsigned v0, v1;
  poff_kmajor(w, lane, S_ * 2, v0, v1);
  gemm64x80<K4_NS, 10, 2, 24, 1, false>(acc, lds0, w, lane, (const char*)D3T + mt * 128, v0, v1, 64 * S_ * 2,
                                        (const char*)&g_FT + (size_t)nt * 5 * 20480, 20480,
                                        (const char*)H + (size_t)(mt * 4 + w) * 49152, lane * 16, 0, 2048,
                                        (const char*)W2P + (size_t)nt * 5 * 49152, 49152);
  const size_t ro = (size_t)row_of(mt * 64 + w * 16 + i) * D_ + nt * 80 + g * 4;
#pragma unroll
  for (int q = 0; q < 5; ++q) {
    const f32x4 v = unpack4(*(const u32x2*)(x + ro + q * 16)) + acc[q];
    *(u32x2*)(out + ro + q * 16) = pack4(v);
  }
}

extern "C" void kernel_launch(void* const* d_in, const int* in_sizes, int n_in, void* d_out, int out_size, void* d_ws, size_t ws_size,
                              hipStream_t stream) {
  const u16* x = (const u16*)d_in[0];
  const u16* wr = (const u16*)d_in[1];
  const u16* wi = (const u16*)d_in[2];
  const u16* w1 = (const u16*)d_in[4];
  const u16* w2 = (const u16*)d_in[5];
  char* ws = (char*)d_ws;
  constexpr size_t XB = (size_t)S_ * D_ * 2;
  u16* X0T = (u16*)(ws + 0 * XB);
  u16* X1T = (u16*)(ws + 1 * XB);
  u16* X2T = (u16*)(ws + 2 * XB);
  u16* X3T = (u16*)(ws + 3 * XB);
  u16* D3T = (u16*)(ws + 4 * XB);
  unsigned* WT = (unsigned*)(ws + 5 * XB);
  u16* W1F = (u16*)(ws + 6 * XB);
  u16* W2F = (u16*)(ws + 7 * XB);
  u16* W2P = (u16*)(ws + 8 * XB);
  u16* H = (u16*)(ws + 9 * XB);
  float* rstd0 = (float*)(ws + 12 * XB);
  float* part = rstd0 + S_;

  (void)hipFuncSetAttribute((const void*)k1_rfft, hipFuncAttributeMaxDynamicSharedMemorySize, K1_NS * G_STAGE);
  (void)hipFuncSetAttribute((const void*)k3_gemm1, hipFuncAttributeMaxDynamicSharedMemorySize, G1_NS * G1_STAGE);
  (void)hipFuncSetAttribute((const void*)k4_gemm2, hipFuncAttributeMaxDynamicSharedMemorySize, K4_NS * G_STAGE);
  (void)hipFuncSetAttribute((const void*)k7_out, hipFuncAttributeMaxDynamicSharedMemorySize, K4_NS * G_STAGE);

  k1_rfft<<<256 + 160, 256, K1_NS * G_STAGE, stream>>>(x, wr, wi, X0T, rstd0, WT);
  k2_cols<<<320 + 384, 256, K0_NS * G_STAGE, stream>>>(X0T, rstd0, WT, X1T, w1, w2, W1F, W2F);
  k3_gemm1<<<256, 512, G1_NS * G1_STAGE, stream>>>(X1T, W1F, H);
  k4_gemm2<<<256, 256, K4_NS * G_STAGE, stream>>>(H, W2F, X1T, X2T, part);
  k5_cols<<<320 + 480, 256, 4 * 8 * 528, stream>>>(X2T, part, WT, X3T, X0T, D3T, w2, W2P);
  k3_gemm1<<<256, 512, G1_NS * G1_STAGE, stream>>>(X3T, W1F, H);
  k7_out<<<256, 256, K4_NS * G_STAGE, stream>>>(H, W2P, D3T, x, (u16*)d_out);
}
```

```cpp
#include <hip/hip_runtime.h>
#include <hip/hip_bf16.h>
#include <stdint.h>
#include <utility>

typedef __bf16 bf16x8 __attribute__((ext_vector_type(8)));
typedef __bf16 bf16x4 __attribute__((ext_vector_type(4)));
typedef __bf16 bf16x2 __attribute__((ext_vector_type(2)));
typedef short s16x4 __attribute__((ext_vector_type(4)));
typedef short s16x8 __attribute__((ext_vector_type(8)));
typedef float f32x4 __attribute__((ext_vector_type(4)));
typedef float f32x2 __attribute__((ext_vector_type(2)));
typedef unsigned u32x4 __attribute__((ext_vector_type(4)));
typedef unsigned u32x2 __attribute__((ext_vector_type(2)));
typedef unsigned short u16;

#define DI __device__ __forceinline__
#define LAS __attribute__((address_space(3)))

#define DBGARG
#define TSDECL
#define TSREF
#define TSPASS
#define TS(I)
#define TSOUT
#define DBGPASS(K)
constexpr int S_ = 2048, D_ = 640, FF_ = 1536;
#define SPAD 64
constexpr int SP_ = S_ + SPAD;
constexpr float EPS_ = 1e-5f;

namespace tab {
constexpr double kPi = 3.14159265358979323846264338327950288;
constexpr double ccos(double x) {
  double x2 = x * x, term = 1.0, sum = 1.0;
  for (int n = 1; n <= 24; ++n) { term *= -x2 / (double)((2 * n - 1) * (2 * n)); sum += term; }
  return sum;
}
constexpr uint16_t tobf(double v) {
  float f = (float)v;
  uint32_t u = __builtin_bit_cast(uint32_t, f);
  u += 0x7fffu + ((u >> 16) & 1u);
  return (uint16_t)(u >> 16);
}
struct CosD { double c[640]; };
constexpr CosD mkcosd() {
  CosD t{};
  for (int j = 0; j < 640; ++j) {
    int jj = j <= 320 ? j : 640 - j;
    t.c[j] = (jj == 160) ? 0.0 : ccos(2.0 * kPi * jj / 640.0);
  }
  return t;
}
constexpr CosD kCd = mkcosd();
constexpr double cs(int n) { return kCd.c[n % 640]; }
constexpr double sn(int n) { return kCd.c[(n + 480) % 640]; }
constexpr double kS2 = 0.05590169943749474241, kS1 = 0.03952847075210474165;
struct Cos { uint16_t c[640]; };
constexpr Cos mkcos() {
  Cos t{};
  for (int j = 0; j < 640; ++j) t.c[j] = tobf(kS2 * kCd.c[j]);
  return t;
}
constexpr Cos kCos = mkcos();
constexpr uint16_t kR = tobf(kS1);
constexpr int bin_of(int cp) {
  int G = cp >> 4, t = (cp >> 3) & 1, j = cp & 7;
  return (t ? (G ? 40 - G : 20) : G) + 40 * j;
}
constexpr uint16_t fval(int d, int kk) {
  int c = bin_of(kk >> 1), p = kk & 1;
  if (c == 0) return (p == 0 && (d & 1)) ? (uint16_t)(kR | 0x8000u) : kR;
  return kCos.c[(d * c + 160 * p) % 640];
}
struct Chunk { uint64_t v[20 * 64 * 2]; };
constexpr Chunk mk(int n16) {
  Chunk ch{};
  for (int k32 = 0; k32 < 20; ++k32)
    for (int l = 0; l < 64; ++l) {
      int n = n16 * 16 + (l & 15), k0 = k32 * 32 + (l >> 4) * 8;
      for (int h = 0; h < 2; ++h) {
        uint64_t w = 0;
        for (int j = 0; j < 4; ++j) w |= (uint64_t)fval(n, k0 + h * 4 + j) << (16 * j);
        ch.v[(k32 * 64 + l) * 2 + h] = w;
      }
    }
  return ch;
}
template <int I> constexpr Chunk cFT = mk(I);
struct Tab { Chunk c[40]; };
template <int... I> constexpr Tab aggT(std::integer_sequence<int, I...>) { return Tab{{cFT<I>...}}; }

constexpr double aval(int r, int d1) {
  if (r >= 40 || d1 >= 40) return 0.0;
  int G = r >> 1, p = r & 1;
  if (G == 0) return (p && (d1 & 1)) ? -0.25 : 0.25;
  return p ? -0.25 * sn(16 * d1 * G) : 0.25 * cs(16 * d1 * G);
}
constexpr double cval(int G, int t, int o, int d2, int p) {
  int j = o >> 1, po = o & 1, c = bin_of(16 * G + 8 * t + j);
  if (G == 0) {
    if (p != t) return 0.0;
    if (c == 0) return (po == 0 && (d2 & 1)) ? -4.0 * kS1 : 4.0 * kS1;
    return 4.0 * kS2 * (po ? -sn(d2 * c) : cs(d2 * c));
  }
  double v = po == 0 ? (p == 0 ? cs(d2 * c) : sn(d2 * c)) : (p == 0 ? -sn(d2 * c) : cs(d2 * c));
  if (t && p) v = -v;
  return 4.0 * kS2 * v;
}
struct Fft { uint16_t c[40][64][8]; uint16_t a[6][64][8]; };
constexpr Fft mkfft() {
  Fft f{};
  for (int l = 0; l < 64; ++l) {
    int g = l >> 4, i = l & 15;
    for (int u = 0; u < 40; ++u)
      for (int e = 0; e < 8; ++e) f.c[u][l][e] = tobf(cval(u >> 1, u & 1, i, 4 * g + (e >> 1), e & 1));
    for (int T = 0; T < 3; ++T) {
      for (int e = 0; e < 8; ++e) f.a[T][l][e] = tobf(aval(16 * T + i, 16 * (e >> 2) + 4 * g + (e & 3)));
      for (int e = 4; e < 8; ++e) f.a[3 + T][l][e] = tobf(aval(16 * T + i, 28 + 4 * g + e));
    }
  }
  return f;
}
}
#if defined(__HIP_DEVICE_COMPILE__)
static __device__ const __attribute__((aligned(1024))) tab::Tab g_FT = tab::aggT(std::make_integer_sequence<int, 40>{});
static __device__ const __attribute__((aligned(1024))) tab::Fft g_FFT = tab::mkfft();
#else
static __device__ const __attribute__((aligned(1024))) tab::Tab g_FT = {};
static __device__ const __attribute__((aligned(1024))) tab::Fft g_FFT = {};
#endif

template <int... I, class F> DI void sfor_impl(std::integer_sequence<int, I...>, F&& f) {
  (f(std::integral_constant<int, I>{}), ...);
}
template <int N, class F> DI void sfor(F&& f) { sfor_impl(std::make_integer_sequence<int, N>{}, f); }

DI f32x4 mfma16(bf16x8 a, bf16x8 b, f32x4 c) { return __builtin_amdgcn_mfma_f32_16x16x32_bf16(a, b, c, 0, 0, 0); }

template <int OFF> DI void glds16(const void* sbase, unsigned voff, unsigned lds_dst) {
  unsigned keep;
  asm volatile("s_mov_b32 %0, m0\n\ts_mov_b32 m0, %3\n\ts_nop 0\n\tglobal_load_lds_dwordx4 %1, %2 offset:%4\n\ts_mov_b32 m0, %0"
               : "=&s"(keep) : "v"(voff), "s"(sbase), "s"(lds_dst), "n"(OFF) : "memory");
}
#define PRE_ 3
constexpr int last_issued(int b, int NS, int T) {
  int l = (PRE_ < NS ? PRE_ : NS) - 1;
  for (int k = 0; k < b; ++k) {
    int n = l + 2;
    if (n > k - 1 + NS) n = k - 1 + NS;
    if (n > l) l = n;
  }
  return l < T - 1 ? l : T - 1;
}
template <int N> DI void wait_bar() {
  asm volatile("s_waitcnt vmcnt(%0) lgkmcnt(0)\n\ts_barrier" ::"n"(N) : "memory");
}
DI void lgkm_bar() { asm volatile("s_waitcnt lgkmcnt(0)\n\ts_barrier" ::: "memory"); }
DI bf16x8 lds128(unsigned addr) { return *(const LAS bf16x8*)(uintptr_t)addr; }
DI bf16x8 lds_tr(unsigned a0, unsigned a1) {
  s16x4 lo = __builtin_amdgcn_ds_read_tr16_b64_v4i16((LAS s16x4*)(uintptr_t)a0);
  s16x4 hi = __builtin_amdgcn_ds_read_tr16_b64_v4i16((LAS s16x4*)(uintptr_t)a1);
  s16x8 v = __builtin_shufflevector(lo, hi, 0, 1, 2, 3, 4, 5, 6, 7);
  return __builtin_bit_cast(bf16x8, v);
}
DI unsigned pack2(float a, float b) {
  f32x2 f = {a, b};
  return __builtin_bit_cast(unsigned, __builtin_convertvector(f, bf16x2));
}
DI u32x2 pack4(f32x4 v) { return u32x2{pack2(v[0], v[1]), pack2(v[2], v[3])}; }
DI bf16x8 pack8(f32x4 a, f32x4 b) {
  u32x4 u = {pack2(a[0], a[1]), pack2(a[2], a[3]), pack2(b[0], b[1]), pack2(b[2], b[3])};
  return __builtin_bit_cast(bf16x8, u);
}
DI float bflo(unsigned u) { return __builtin_bit_cast(float, u << 16); }
DI float bfhi(unsigned u) { return __builtin_bit_cast(float, u & 0xffff0000u); }
DI f32x4 unpack4(u32x2 u) { return f32x4{bflo(u[0]), bfhi(u[0]), bflo(u[1]), bfhi(u[1])}; }
DI bf16x8 neg8(bf16x8 v) {
  u32x4 u = __builtin_bit_cast(u32x4, v);
  u ^= 0x80008000u;
  return __builtin_bit_cast(bf16x8, u);
}
DI int row_of(int m) { return 32 * (m & 63) + (m >> 6); }
DI int uni(int v) { return __builtin_amdgcn_readfirstlane(v); }
template <class T> DI void wst(void* p, T v) {
  static_assert(sizeof(T) == 16 || sizeof(T) == 8, "");
  if constexpr (sizeof(T) == 16) asm volatile("global_store_dwordx4 %0, %1, off sc1\n\ts_nop 1" ::"v"(p), "v"(v) : "memory");
  else asm volatile("global_store_dwordx2 %0, %1, off sc1" ::"v"(p), "v"(v) : "memory");
}

constexpr int G_QOFF = 8192;
constexpr int G_STAGE5 = 18432, G_STAGE4 = 16384;
#define KSP true

struct PSrc {
  const char* base;
  unsigned v0, v1;
  unsigned step;
};

template <int PKIND> struct PRead {
  unsigned a0, a1;
};

template <int OFF> DI void gld16(bf16x8& dst, const void* sbase, unsigned voff) {
  asm volatile("global_load_dwordx4 %0, %1, %2 offset:%3" : "=v"(dst) : "v"(voff), "s"(sbase), "n"(OFF) : "memory");
}
template <int N> DI void qwait(bf16x8& a, bf16x8& b) { asm volatile("s_waitcnt vmcnt(%2)" : "+v"(a), "+v"(b) : "n"(N) : "memory"); }
template <int N> DI void qwait(bf16x8& a, bf16x8& b, bf16x8& c) {
  asm volatile("s_waitcnt vmcnt(%3)" : "+v"(a), "+v"(b), "+v"(c) : "n"(N) : "memory");
}
template <int N, int NT> DI void qwaitv(bf16x8 (&f)[NT]) {
  if constexpr (NT == 2) qwait<N>(f[0], f[1]); else qwait<N>(f[0], f[1], f[2]);
}

template <int NS, int T1, int PK1, int T2, int PK2>
DI void p_loader(unsigned lds0, int w, int lane, const char* p1base, unsigned p1v0, unsigned p1v1, unsigned p1step,
                 const char* p2base, unsigned p2v0, unsigned p2v1, unsigned p2step) {
  constexpr int T = T1 + T2;
  auto issue = [&](auto tc) {
    constexpr int t = decltype(tc)::value;
    const unsigned slot = lds0 + (t % NS) * 8192;
    constexpr bool ph1 = t < T1;
    constexpr int tt = ph1 ? t : t - T1;
    constexpr int PK = ph1 ? PK1 : PK2;
    const char* pb = (ph1 ? p1base : p2base) + (size_t)tt * (ph1 ? p1step : p2step);
    if constexpr (PK == 1) {
      glds16<0>(pb, ph1 ? p1v0 : p2v0, slot + w * 2048);
      glds16<1024>(pb, ph1 ? p1v0 : p2v0, slot + w * 2048);
    } else {
      glds16<0>(pb, ph1 ? p1v0 : p2v0, slot + w * 2048);
      glds16<0>(pb, ph1 ? p1v1 : p2v1, slot + w * 2048 + 1024);
    }
  };
  sfor<last_issued(0, NS, T) + 1>(issue);
  sfor<T>([&](auto tc) {
    constexpr int b = decltype(tc)::value;
    constexpr int last = last_issued(b, NS, T), nxt = last_issued(b + 1, NS, T);
    wait_bar<2 * (last - b)>();
    sfor<nxt - last>([&](auto kc) { issue(std::integral_constant<int, last + 1 + decltype(kc)::value>{}); });
  });
}
template <int PK> DI bf16x8 p_read(unsigned slot, int j, int ks, int lane) {
  const int g = lane >> 4, i = lane & 15, q4 = i >> 2, p4 = i & 3;
  if constexpr (PK == 0) return lds128(slot + j * 2048 + i * 128 + ((unsigned)((4 * ks + g) ^ (i & 7)) << 4));
  else if constexpr (PK == 1) return lds128(slot + j * 2048 + ks * 1024 + lane * 16);
  else {
    const unsigned keyT = ((q4 >> 1) & 1) | ((g & 1) << 1);
    const unsigned a = slot + (8 * g + q4) * 128 + (((unsigned)(2 * j + (p4 >> 1)) ^ (keyT << 1)) << 4) + 8 * (p4 & 1) + ks * 4096;
    return lds_tr(a, a + 512);
  }
}

template <int NQ, int NS, int DQ, int T1, int PK1, int T2, int PK2, bool P_IS_A>
DI bool gemm64(f32x4 (&out)[NQ], unsigned lds0, int wave, int lane,
               const char* p1base, unsigned p1v0, unsigned p1v1, unsigned p1step, const char* q1base, unsigned q1stride,
               const char* p2base, unsigned p2v0, unsigned p2v1, unsigned p2step, const char* q2base, unsigned q2stride TSREF) {
  constexpr int T = T1 + T2, NQA = (NQ + 1) / 2;
  const int w = wave & 3;
  if (wave >= 4) {
    p_loader<NS, T1, PK1, T2, PK2>(lds0, w, lane, p1base, p1v0, p1v1, p1step, p2base, p2v0, p2v1, p2step);
    return false;
  }
  const int nh = w >> 1, km = w & 1;
  const unsigned qv = lane * 16 + km * 1024;
  auto body = [&](auto ntc) {
    constexpr int NT = decltype(ntc)::value;
    const int q0 = nh * NQA;
    const char* qa = q1base + (size_t)q0 * q1stride;
    const char* qb = q2base + (size_t)q0 * q2stride;
    bf16x8 pf[2][4], qf[DQ + 2][NT];
    f32x4 a2[4][NT] = {};
    auto qissue = [&](auto tc) {
      constexpr int t = decltype(tc)::value;
#pragma unroll
      for (int q = 0; q < NT; ++q) {
        if constexpr (t < T1) gld16<0>(qf[t % (DQ + 2)][q], qa + (size_t)q * q1stride + t * 2048, qv);
        else gld16<0>(qf[t % (DQ + 2)][q], qb + (size_t)q * q2stride + (t - T1) * 2048, qv);
      }
    };
    auto mm = [&](auto tc, auto lc) {
      constexpr int t = decltype(tc)::value, last = decltype(lc)::value;
      qwaitv<NT*(last - t), NT>(qf[t % (DQ + 2)]);
#pragma unroll
      for (int q = 0; q < NT; ++q)
#pragma unroll
        for (int c = 0; c < 4; ++c)
          a2[c][q] = P_IS_A ? mfma16(pf[t & 1][c], qf[t % (DQ + 2)][q], a2[c][q]) : mfma16(qf[t % (DQ + 2)][q], pf[t & 1][c], a2[c][q]);
    };
    TS(1);
    sfor<(DQ < T ? DQ : T)>(qissue);
    sfor<T>([&](auto tc) {
      constexpr int b = decltype(tc)::value;
      lgkm_bar();
      TS(8 + b);
      const unsigned slot = lds0 + (b % NS) * 8192;
#pragma unroll
      for (int c = 0; c < 4; ++c) pf[b & 1][c] = p_read<(b < T1 ? PK1 : PK2)>(slot, c, km, lane);
      if constexpr (b + DQ < T) qissue(std::integral_constant<int, b + DQ>{});
      __builtin_amdgcn_sched_barrier(0);
      if constexpr (b >= 1) mm(std::integral_constant<int, b - 1>{}, std::integral_constant<int, (b + DQ < T ? b + DQ : T - 1)>{});
      __builtin_amdgcn_sched_barrier(0);
    });
    mm(std::integral_constant<int, T - 1>{}, std::integral_constant<int, T - 1>{});
    lgkm_bar();
    const unsigned ex = lds0 + (km * 4 * NQ + q0) * 1024 + lane * 16;
#pragma unroll
    for (int c = 0; c < 4; ++c)
#pragma unroll
      for (int q = 0; q < NT; ++q) *(LAS f32x4*)(uintptr_t)(ex + (c * NQ + q) * 1024) = a2[c][q];
  };
  if constexpr (NQ & 1) {
    if (nh == 0) body(std::integral_constant<int, NQA>{}); else body(std::integral_constant<int, NQ - NQA>{});
  } else {
    body(std::integral_constant<int, NQA>{});
  }
  lgkm_bar();
  const unsigned ex = lds0 + w * NQ * 1024 + lane * 16;
#pragma unroll
  for (int q = 0; q < NQ; ++q)
    out[q] = *(const LAS f32x4*)(uintptr_t)(ex + q * 1024) + *(const LAS f32x4*)(uintptr_t)(ex + (4 * NQ + q) * 1024);
  return true;
}

DI void poff_rows(int w, int lane, int row0, bool permuted, unsigned& v0, unsigned& v1) {
  const int r = lane >> 3, ch = (lane & 7) ^ (r & 7);
  int ra = row0 + 16 * w + r, rb = ra + 8;
  if (permuted) { ra = row_of(ra); rb = row_of(rb); }
  v0 = ra * 1280 + ch * 16;
  v1 = rb * 1280 + ch * 16;
}
DI void poff_kmajor(int w, int lane, unsigned rowbytes, unsigned& v0, unsigned& v1) {
  const int r = lane >> 3, chp = lane & 7;
  const int row_a = 16 * w + r, row_b = row_a + 8;
  const unsigned ka = ((row_a >> 1) & 1) | (((row_a >> 3) & 1) << 1), kb = ((row_b >> 1) & 1) | (((row_b >> 3) & 1) << 1);
  v0 = row_a * rowbytes + ((chp ^ (ka << 1)) << 4);
  v1 = row_b * rowbytes + ((chp ^ (kb << 1)) << 4);
}

constexpr int RP = 1296;
typedef short s16x12 __attribute__((ext_vector_type(12)));
DI s16x4 lds_tr4(unsigned a) { return __builtin_amdgcn_ds_read_tr16_b64_v4i16((LAS s16x4*)(uintptr_t)a); }
template <int CTRL> DI float dpp_add(float v) {
  return v + __builtin_bit_cast(float, __builtin_amdgcn_update_dpp(0, __builtin_bit_cast(int, v), CTRL, 0xf, 0xf, true));
}
DI float row16_sum(float v) {
  return dpp_add<0x121>(dpp_add<0x122>(dpp_add<0x124>(dpp_add<0x128>(v))));
}
DI void fft_step_a(f32x4 (&a)[3], const bf16x8 (&ta)[6], unsigned a0, unsigned a1, unsigned a2) {
  const s16x4 z0 = lds_tr4(a0), z1 = lds_tr4(a1), z2 = lds_tr4(a2);
  const bf16x8 lo = __builtin_bit_cast(bf16x8, __builtin_shufflevector(z0, z1, 0, 1, 2, 3, 4, 5, 6, 7));
  const bf16x8 hi = __builtin_bit_cast(bf16x8, __builtin_shufflevector(z1, z2, 0, 1, 2, 3, 4, 5, 6, 7));
#pragma unroll
  for (int T = 0; T < 3; ++T) a[T] = mfma16(ta[T], lo, f32x4{});
#pragma unroll
  for (int T = 0; T < 3; ++T) a[T] = mfma16(ta[3 + T], hi, a[T]);
}

template <int MODE>
DI void fft640_block(int blk, unsigned lds0, const u16* __restrict__ src, u16* __restrict__ dst, float* __restrict__ part TSREF) {
  const int lane = threadIdx.x & 63, w = uni(threadIdx.x >> 6), g = lane >> 4, i = lane & 15;
  const unsigned lane16 = lane * 16;
  if constexpr (MODE != 2) {
#pragma unroll
    for (int r = 0; r < 4; ++r) {
      const int rho = 4 * w + r;
      const int grow = MODE == 0 ? row_of(blk * 32 + 8 * (w & 3) + 4 * (w >> 2) + r) : blk * 32 + rho;
      glds16<0>((const char*)src + (size_t)grow * 1280, lane16, lds0 + rho * RP);
    }
    if (lane < 16) {
#pragma unroll
      for (int r = 0; r < 4; ++r) {
        const int rho = 4 * w + r;
        const int grow = MODE == 0 ? row_of(blk * 32 + 8 * (w & 3) + 4 * (w >> 2) + r) : blk * 32 + rho;
        glds16<1024>((const char*)src + (size_t)grow * 1280, lane16, lds0 + rho * RP);
      }
    }
  } else {
    const unsigned vo = (lane >> 2) * (FF_ * 2) + (lane & 3) * 16;
#pragma unroll
    for (int p = 0; p < 5; ++p)
      glds16<0>((const char*)src + blk * 64 + (size_t)(5 * w + p) * (16 * FF_ * 2), vo, lds0 + (5 * w + p) * 1088);
  }
  if constexpr (MODE == 3) {
    wait_bar<0>();
    const unsigned rb = lds0 + (4 * g + (i >> 2)) * RP + (i & 3) * 8 + w * 160;
#pragma unroll
    for (int q = 0; q < 5; ++q)
      wst((char*)dst + ((size_t)((5 * w + q) * 48 + blk) * 64 + lane) * 16, lds_tr(rb + q * 32, rb + q * 32 + 16 * RP));
    return;
  }
  const char* tb = (const char*)&g_FFT;
  bf16x8 ta[6], tc[5];
  sfor<6>([&](auto uc) { constexpr int u = decltype(uc)::value; gld16<(u & 3) * 1024>(ta[u], tb + 40960 + (u >> 2) * 4096, lane16); });
  sfor<5>([&](auto uc) { constexpr int u = decltype(uc)::value; gld16<(u & 3) * 1024>(tc[u], tb + w * 5120 + (u >> 2) * 4096, lane16); });
  asm volatile("s_waitcnt vmcnt(5)" : "+v"(ta[0]), "+v"(ta[1]), "+v"(ta[2]), "+v"(ta[3]), "+v"(ta[4]), "+v"(ta[5])::"memory");
  if constexpr (MODE == 2) lgkm_bar();
  TS(1);
  if constexpr (MODE < 2) {
    f32x4 a[4][3];
#pragma unroll
    for (int r = 0; r < 4; ++r) {
      const unsigned rb = lds0 + (4 * w + r) * RP;
      fft_step_a(a[r], ta, rb + lane * 8, rb + 512 + lane * 8, rb + 1024 + (lane & 31) * 8);
    }
    const unsigned wb = lds0 + 4 * w * RP + g * 128 + i * 4;
#pragma unroll
    for (int r = 0; r < 4; ++r)
#pragma unroll
      for (int T = 0; T < 2; ++T) {
        *(LAS unsigned*)(uintptr_t)(wb + r * RP + T * 512) = pack2(a[r][T][0], a[r][T][1]);
        *(LAS unsigned*)(uintptr_t)(wb + r * RP + T * 512 + 64) = pack2(a[r][T][2], a[r][T][3]);
      }
    if (g < 2) {
#pragma unroll
      for (int r = 0; r < 4; ++r) {
        *(LAS unsigned*)(uintptr_t)(wb + r * RP + 1024) = pack2(a[r][2][0], a[r][2][1]);
        *(LAS unsigned*)(uintptr_t)(wb + r * RP + 1024 + 64) = pack2(a[r][2][2], a[r][2][3]);
      }
    }
  } else {
    const int j = w & 3, ft = w >> 2;
    const unsigned rb = lds0 + (4 * g + (i >> 2)) * 1088 + j * 256 + (i & 3) * 8 + ft * 32;
    const unsigned rt = lds0 + (32 + 4 * (g & 1) + (i >> 2)) * 1088 + j * 256 + (i & 3) * 8 + ft * 32;
    f32x4 a[4][3];
#pragma unroll
    for (int dd = 0; dd < 4; ++dd) fft_step_a(a[dd], ta, rb + dd * 64, rb + dd * 64 + 16 * 1088, rt + dd * 64);
    u32x4 u[3][2];
#pragma unroll
    for (int T = 0; T < 3; ++T)
#pragma unroll
      for (int dd = 0; dd < 4; ++dd) {
        u[T][0][dd] = pack2(a[dd][T][0], a[dd][T][1]);
        u[T][1][dd] = pack2(a[dd][T][2], a[dd][T][3]);
      }
    lgkm_bar();
    const unsigned wb = lds0 + (16 * ft + i) * RP + g * 128 + j * 16;
#pragma unroll
    for (int T = 0; T < 3; ++T)
      if (T < 2 || g < 2) {
        *(LAS u32x4*)(uintptr_t)(wb + T * 512) = u[T][0];
        *(LAS u32x4*)(uintptr_t)(wb + T * 512 + 64) = u[T][1];
      }
  }
  TS(2);
  lgkm_bar();
  asm volatile("s_waitcnt vmcnt(0)" : "+v"(tc[0]), "+v"(tc[1]), "+v"(tc[2]), "+v"(tc[3]), "+v"(tc[4])::"memory");
  TS(3);
  const unsigned db = lds0 + i * RP + g * 16;
  f32x4 sq[2] = {};
#pragma unroll
  for (int q = 0; q < 5; ++q) {
    const int u = 5 * w + q, G = u >> 1;
    const bf16x8 d0 = lds128(db + G * 64), d1 = lds128(db + 16 * RP + G * 64);
    if constexpr (MODE < 2) {
      const f32x4 a = mfma16(d0, tc[q], f32x4{}), b = mfma16(d1, tc[q], f32x4{});
      const bf16x8 o = pack8(a, b);
      if constexpr (MODE == 0) {
        wst(dst + (size_t)(16 * u + i) * SP_ + blk * 32 + 8 * g, o);
        if (q != 0 || w != 0 || i != 1) { sq[0] += a * a; sq[1] += b * b; }
      } else {
        wst((char*)dst + ((size_t)(u * 48 + blk) * 64 + lane) * 16, o);
      }
    } else {
      f32x4 a = mfma16(tc[q], d0, f32x4{}), b = mfma16(tc[q], d1, f32x4{});
      if (q == 0 && w == 0 && g == 0) { a[1] = 0.f; b[1] = 0.f; }
      char* o = (char*)dst + ((size_t)(2 * blk * 20 + G) * 64 + (2 * (u & 1) + (g >> 1)) * 16 + i) * 16 + (g & 1) * 8;
      wst(o, pack4(a));
      wst(o + 20480, pack4(b));
    }
  }
  if constexpr (MODE == 0) {
#pragma unroll
    for (int r = 0; r < 4; ++r) { sq[0][r] = row16_sum(sq[0][r]); sq[1][r] = row16_sum(sq[1][r]); }
    if (i < 2) wst(part + w * S_ + blk * 32 + 8 * g + 4 * i, i ? sq[1] : sq[0]);
  }
}

constexpr int K1_X = 64, K1_WT = 160;
__global__ __launch_bounds__(512) void k1_prep(const u16* __restrict__ x, const u16* __restrict__ wr, const u16* __restrict__ wi,
                                              u16* __restrict__ X0T, float* __restrict__ part, unsigned* __restrict__ WT DBGARG) {
  TSDECL; TS(0);
  extern __shared__ __attribute__((aligned(16))) char smem[];
  const unsigned lds0 = (unsigned)(uintptr_t)(LAS char*)smem;
  const int tid = threadIdx.x;
  int b = blockIdx.x;
  if (b < K1_X) {
    fft640_block<0>(b, lds0, x, X0T, part TSPASS);
  } else {
    b -= K1_X;
    const int kt = b % 10, ot = b / 10;
    const int kc = tid & 31, r = tid >> 5, e = r & 7;
    const int t0 = 16 * (ot >> 2) + (r >> 3) + 64 * (16 * (e >> 2) + 4 * (ot & 3) + (e & 3));
    const u16* pr = wr + t0 * 321 + kt * 32 + kc + 1;
    const u16* pi = wi + t0 * 321 + kt * 32 + kc + 1;
    unsigned v[8];
#pragma unroll
    for (int it = 0; it < 8; ++it) v[it] = (unsigned)pr[it * 642] | ((unsigned)pi[it * 642] << 16);
    const unsigned wa = lds0 + (kc * 132 + r) * 4;
#pragma unroll
    for (int it = 0; it < 8; ++it) *(LAS unsigned*)(uintptr_t)(wa + it * 64) = v[it];
    lgkm_bar();
#pragma unroll
    for (int it = 0; it < 2; ++it) {
      const int kc2 = r + 16 * it;
      const int c = (kt * 32 + kc2 + 1) % 320, rr = c % 40, j = c / 40;
      const int cp = (rr == 0 ? 0 : rr == 20 ? 8 : rr < 20 ? 16 * rr : 16 * (40 - rr) + 8) + j;
      wst(WT + cp * 2048 + ot * 128 + kc * 4, *(const LAS u32x4*)(uintptr_t)(lds0 + (kc2 * 132 + kc * 4) * 4));
    }
  }
  TSOUT;
}

template <int OFF, class T> DI void gldv(T& dst, const void* sbase, unsigned voff) {
  asm volatile("global_load_dwordx4 %0, %1, %2 offset:%3" : "=v"(dst) : "v"(voff), "s"(sbase), "n"(OFF) : "memory");
}
struct Cx { float c, s; };
DI Cx cis(float rev) { return Cx{__builtin_amdgcn_cosf(rev), __builtin_amdgcn_sinf(rev)}; }
DI Cx cmul(Cx a, Cx b) { return Cx{a.c * b.c - a.s * b.s, a.c * b.s + a.s * b.c}; }
constexpr int C_CH = 528, C_PL = 8 * C_CH, C_OP = 32 * 272;
constexpr int C_MID = 2 * C_OP, C_HALF = C_MID + 2 * C_PL, C_RS = 2 * C_HALF, C_LDS = C_RS + 8192;
#define PDELAY 0
template <bool SECOND>
__global__ __launch_bounds__(512) void k2_cols(const u16* __restrict__ Xin, const float* __restrict__ part, const unsigned* __restrict__ WT,
                                              u16* __restrict__ Xout, const u16* __restrict__ wa, u16* __restrict__ Wa,
                                              const u16* __restrict__ wb, u16* __restrict__ Wb DBGARG) {
  TSDECL; TS(0);
  extern __shared__ __attribute__((aligned(16))) char smem[];
  if (blockIdx.x >= 160) {
    const unsigned l0 = (unsigned)(uintptr_t)(LAS char*)smem;
    const int b = blockIdx.x - 160;
    if (PDELAY) __builtin_amdgcn_s_sleep(PDELAY);
    if constexpr (SECOND) fft640_block<3>(b, l0, wa, Wa, nullptr TSPASS);
    else if (b < 48) fft640_block<2>(b, l0, wa, Wa, nullptr TSPASS);
    else fft640_block<1>(b - 48, l0, wb, Wb, nullptr TSPASS);
    TSOUT;
    return;
  }
  const int half = uni(threadIdx.x >> 8), c = 2 * blockIdx.x + half;
  const unsigned ldsb = (unsigned)(uintptr_t)(LAS char*)smem, lds0 = ldsb + half * C_HALF;
  const int tid = threadIdx.x & 255, lane = tid & 63, w = uni(tid >> 6), g = lane >> 4, i = lane & 15;
  f32x4 ps[8];
  u32x4 xin[2], wv[2];
  gldv<0>(xin[0], Xin + (size_t)(2 * c) * SP_, tid * 16);
  gldv<0>(xin[1], Xin + (size_t)(2 * c + 1) * SP_, tid * 16);
#pragma unroll
  for (int p = 0; p < 8; ++p) gldv<0>(ps[p], part + p * S_, threadIdx.x * 16);
  gldv<0>(wv[0], WT + (size_t)c * 2048, tid * 32);
  gldv<16>(wv[1], WT + (size_t)c * 2048, tid * 32);
  __builtin_amdgcn_sched_barrier(0);
  bf16x8 f1r[2], f1i[2], f1n[2], f2r[2], f2i[2], f2n[2];
  const int k1c = 16 * w + i;
  const unsigned odd = (i & 1) ? 0x80008000u : 0u;
  {
    const Cx st = cis((float)k1c * (-1.f / 64.f));
    Cx v = cis((float)((g * k1c) & 7) * (-1.f / 8.f));
    v.c *= 0.125f; v.s *= 0.125f;
    f32x4 cr[2], ci[2];
#pragma unroll
    for (int j = 0; j < 8; ++j) {
      cr[j >> 2][j & 3] = v.c; ci[j >> 2][j & 3] = v.s;
      if (j < 7) v = cmul(v, st);
    }
    f1r[0] = pack8(cr[0], cr[1]);
    f1i[0] = pack8(ci[0], ci[1]);
    f1r[1] = __builtin_bit_cast(bf16x8, __builtin_bit_cast(u32x4, f1r[0]) ^ odd);
    f1i[1] = __builtin_bit_cast(bf16x8, __builtin_bit_cast(u32x4, f1i[0]) ^ odd);
    f1n[0] = neg8(f1i[0]);
    f1n[1] = neg8(f1i[1]);
  }
  {
    const Cx st = cis((float)i * (-1.f / 32.f));
    Cx v = cis((float)((g * i) & 7) * (-1.f / 8.f));
    v.c *= 0.17677669529663688f; v.s *= 0.17677669529663688f;
    f32x4 cr, ci;
#pragma unroll
    for (int j = 0; j < 4; ++j) {
      cr[j] = v.c; ci[j] = v.s;
      if (j < 3) v = cmul(v, st);
    }
    const u32x2 pr = pack4(cr), pi = pack4(ci);
    const u32x4 r0 = {pr[0], pr[1], pr[0] ^ odd, pr[1] ^ odd}, i0 = {pi[0], pi[1], pi[0] ^ odd, pi[1] ^ odd};
    f2r[0] = __builtin_bit_cast(bf16x8, r0);
    f2i[0] = __builtin_bit_cast(bf16x8, i0);
    f2r[1] = __builtin_bit_cast(bf16x8, r0 ^ 0x80000000u);
    f2i[1] = __builtin_bit_cast(bf16x8, i0 ^ 0x80000000u);
    f2n[0] = neg8(f2i[0]);
    f2n[1] = neg8(f2i[1]);
  }
  f32x4 twc[2], tws[2], tvc[2], tvs[2];
  {
    const Cx st = cis((float)k1c * (1.f / 2048.f)), jp = cis((float)k1c * (1.f / 128.f));
    Cx v = cis((float)(g * k1c) * (1.f / 512.f));
#pragma unroll
    for (int r = 0; r < 4; ++r) {
      const Cx u = cmul(v, jp);
      twc[0][r] = v.c; tws[0][r] = v.s; twc[1][r] = u.c; tws[1][r] = u.s;
      if (r < 3) v = cmul(v, st);
    }
#pragma unroll
    for (int mt = 0; mt < 2; ++mt) {
      const Cx s2 = cis((float)(16 * mt + i) * (1.f / 2048.f));
      Cx u = cis((float)((16 * mt + i) * (4 * w + g)) * (1.f / 512.f));
#pragma unroll
      for (int r = 0; r < 4; ++r) {
        tvc[mt][r] = u.c; tvs[mt][r] = u.s;
        if (r < 3) u = cmul(u, s2);
      }
    }
  }
  asm volatile("" : "+v"(f1r[0]), "+v"(f1r[1]), "+v"(f1i[0]), "+v"(f1i[1]), "+v"(f1n[0]), "+v"(f1n[1]), "+v"(f2r[0]), "+v"(f2r[1]),
               "+v"(f2i[0]), "+v"(f2i[1]), "+v"(f2n[0]), "+v"(f2n[1]));
  asm volatile("" : "+v"(twc[0]), "+v"(twc[1]), "+v"(tws[0]), "+v"(tws[1]), "+v"(tvc[0]), "+v"(tvc[1]), "+v"(tvs[0]), "+v"(tvs[1]));
  TS(1);
  asm volatile("s_waitcnt vmcnt(2)" : "+v"(ps[0]), "+v"(ps[1]), "+v"(ps[2]), "+v"(ps[3]), "+v"(ps[4]), "+v"(ps[5]), "+v"(ps[6]), "+v"(ps[7]),
               "+v"(xin[0]), "+v"(xin[1])::"memory");
  {
    f32x4 a = ((ps[0] + ps[1]) + (ps[2] + ps[3])) + ((ps[4] + ps[5]) + (ps[6] + ps[7]));
#pragma unroll
    for (int e = 0; e < 4; ++e) a[e] = rsqrtf(a[e] * (1.f / D_) + EPS_);
    *(LAS f32x4*)(uintptr_t)(ldsb + C_RS + (threadIdx.x & 1) * 4096 + (threadIdx.x >> 1) * 16) = a;
  }
  lgkm_bar();
  {
    const f32x4 ra = *(const LAS f32x4*)(uintptr_t)(ldsb + C_RS + tid * 16), rb = *(const LAS f32x4*)(uintptr_t)(ldsb + C_RS + 4096 + tid * 16);
    const unsigned ad = lds0 + (tid & 7) * C_CH + (tid >> 3) * 16;
#pragma unroll
    for (int q = 0; q < 2; ++q) {
      u32x4 a = {pack2(bflo(xin[q][0]) * ra[0], bfhi(xin[q][0]) * ra[1]), pack2(bflo(xin[q][1]) * ra[2], bfhi(xin[q][1]) * ra[3]),
                 pack2(bflo(xin[q][2]) * rb[0], bfhi(xin[q][2]) * rb[1]), pack2(bflo(xin[q][3]) * rb[2], bfhi(xin[q][3]) * rb[3])};
      if (q == 1 && c == 0) a = u32x4{0, 0, 0, 0};
      *(LAS u32x4*)(uintptr_t)(ad + q * C_PL) = a;
    }
  }
  asm volatile("s_waitcnt vmcnt(0) lgkmcnt(0)\n\ts_barrier" : "+v"(wv[0]), "+v"(wv[1])::"memory");
  TS(2);
  const unsigned rd = lds0 + g * C_CH + i * 16;
  {
    f32x4 ar[2] = {}, ai[2] = {};
#pragma unroll
    for (int mt = 0; mt < 2; ++mt)
#pragma unroll
      for (int ks = 0; ks < 2; ++ks) {
        const bf16x8 ur = lds128(rd + ks * 4 * C_CH + mt * 256), ui = lds128(rd + C_PL + ks * 4 * C_CH + mt * 256);
        ar[mt] = mfma16(ur, f1r[ks], ar[mt]);
        ai[mt] = mfma16(ur, f1i[ks], ai[mt]);
        ar[mt] = mfma16(ui, f1n[ks], ar[mt]);
        ai[mt] = mfma16(ui, f1r[ks], ai[mt]);
      }
    f32x4 br[2], bi[2];
#pragma unroll
    for (int mt = 0; mt < 2; ++mt) {
      br[mt] = ar[mt] * twc[mt] + ai[mt] * tws[mt];
      bi[mt] = ai[mt] * twc[mt] - ar[mt] * tws[mt];
    }
    bf16x8 pr = pack8(br[0], br[1]), pi = pack8(bi[0], bi[1]);
    f32x4 yr[2] = {}, yi[2] = {};
#pragma unroll
    for (int mt = 0; mt < 2; ++mt) {
      yr[mt] = mfma16(f2r[mt], pr, yr[mt]);
      yi[mt] = mfma16(f2i[mt], pr, yi[mt]);
      yr[mt] = mfma16(f2n[mt], pi, yr[mt]);
      yi[mt] = mfma16(f2r[mt], pi, yi[mt]);
    }
#pragma unroll
    for (int mt = 0; mt < 2; ++mt) {
      const u32x4 ww = wv[mt];
#pragma unroll
      for (int r = 0; r < 4; ++r) {
        const float wr_ = bflo(ww[r]), wi_ = bfhi(ww[r]);
        br[mt][r] = yr[mt][r] * wr_ - yi[mt][r] * wi_;
        bi[mt][r] = yr[mt][r] * wi_ + yi[mt][r] * wr_;
      }
    }
    pr = pack8(br[0], br[1]);
    pi = pack8(bi[0], bi[1]);
    f32x4 zr[2] = {}, zi[2] = {};
#pragma unroll
    for (int nt = 0; nt < 2; ++nt) {
      zr[nt] = mfma16(pr, f2r[nt], zr[nt]);
      zi[nt] = mfma16(pi, f2r[nt], zi[nt]);
      zr[nt] = mfma16(pi, f2i[nt], zr[nt]);
      zi[nt] = mfma16(pr, f2n[nt], zi[nt]);
    }
    const unsigned ad = lds0 + C_MID + (2 * w + (g >> 1)) * C_CH + i * 16 + (g & 1) * 8;
#pragma unroll
    for (int nt = 0; nt < 2; ++nt) {
      const f32x4 a = zr[nt] * tvc[nt] - zi[nt] * tvs[nt];
      const f32x4 b = zi[nt] * tvc[nt] + zr[nt] * tvs[nt];
      *(LAS u32x2*)(uintptr_t)(ad + nt * 256) = pack4(a);
      *(LAS u32x2*)(uintptr_t)(ad + C_PL + nt * 256) = pack4(b);
    }
  }
  lgkm_bar();
  TS(3);
  {
    const unsigned r2 = rd + C_MID;
    const unsigned ad = lds0 + i * 272 + w * 64 + g * 16;
#pragma unroll
    for (int nt = 0; nt < 2; ++nt) {
      f32x4 vr = {}, vi = {};
#pragma unroll
      for (int ks = 0; ks < 2; ++ks) {
        const bf16x8 qr = lds128(r2 + ks * 4 * C_CH + nt * 256), qi = lds128(r2 + C_PL + ks * 4 * C_CH + nt * 256);
        vr = mfma16(f1r[ks], qr, vr);
        vi = mfma16(f1r[ks], qi, vi);
        vr = mfma16(f1i[ks], qi, vr);
        vi = mfma16(f1n[ks], qr, vi);
      }
      *(LAS f32x4*)(uintptr_t)(ad + nt * 16 * 272) = vr;
      *(LAS f32x4*)(uintptr_t)(ad + C_OP + nt * 16 * 272) = vi;
    }
  }
  lgkm_bar();
  TS(4);
  const unsigned ro = lds0 + (tid >> 3) * 272 + (tid & 7) * 32;
#pragma unroll
  for (int q = 0; q < 2; ++q) {
    f32x4 a = *(const LAS f32x4*)(uintptr_t)(ro + q * C_OP), b = *(const LAS f32x4*)(uintptr_t)(ro + q * C_OP + 16);
    if (q == 1 && c == 0) { a = f32x4{}; b = f32x4{}; }
    a += unpack4(u32x2{xin[q][0], xin[q][1]});
    b += unpack4(u32x2{xin[q][2], xin[q][3]});
    wst(Xout + (size_t)(2 * c + q) * SP_ + tid * 8, pack8(a, b));
  }
  TSOUT;
}

#define DQ3_ 2
constexpr int G1_NS = 8, DQ3 = DQ3_;
__global__ __launch_bounds__(512) void k3_gemm1(const u16* __restrict__ XT, const u16* __restrict__ W1F, u16* __restrict__ H DBGARG) {
  TSDECL; TS(0);
  extern __shared__ __attribute__((aligned(16))) char smem[];
  const unsigned lds0 = (unsigned)(uintptr_t)(LAS char*)smem;
  const int tid = threadIdx.x, lane = tid & 63, wave = uni(tid >> 6), w = wave & 3, g = lane >> 4, i = lane & 15;
  const int b = blockIdx.x, xcd = b & 7, j = b >> 3;
  const int mt = (xcd >> 1) * 8 + (j & 7), ft = (xcd & 1) * 4 + (j >> 3);
  constexpr int T = 10, NS = G1_NS, DQ = DQ3;
  if (wave >= 4) {
    unsigned v0, v1;
    poff_kmajor(w, lane, SP_ * 2, v0, v1);
    p_loader<NS, T, 2, 0, 0>(lds0, w, lane, (const char*)XT + mt * 128, v0, v1, 64 * SP_ * 2, nullptr, 0, 0, 0);
    return;
  }
  const char* wb = (const char*)W1F + (size_t)(ft * 12 + w * 3) * 20480;
  const unsigned lane16 = lane * 16;
  f32x4 acc[3][4] = {};
  f32x4 dg = {};
  bf16x8 xf[2][4], wq[DQ + 2][2][3];
  auto qissue = [&](auto tc) {
    constexpr int t = decltype(tc)::value;
#pragma unroll
    for (int q = 0; q < 3; ++q) gld16<0>(wq[t % (DQ + 2)][0][q], wb + q * 20480 + t * 2048, lane16);
#pragma unroll
    for (int q = 0; q < 3; ++q) gld16<1024>(wq[t % (DQ + 2)][1][q], wb + q * 20480 + t * 2048, lane16);
  };
  auto rd = [&](auto tc, auto kc) {
    constexpr int t = decltype(tc)::value, ks = decltype(kc)::value;
#pragma unroll
    for (int c = 0; c < 4; ++c) xf[ks][c] = p_read<2>(lds0 + (t % NS) * 8192, c ^ w, ks, lane);
  };
  auto mm = [&](auto tc, auto kc, auto lc) {
    constexpr int t = decltype(tc)::value, ks = decltype(kc)::value, last = decltype(lc)::value;
    qwaitv<6 * (last - t) + (ks ? 0 : 3), 3>(wq[t % (DQ + 2)][ks]);
#pragma unroll
    for (int q = 0; q < 3; ++q)
#pragma unroll
      for (int c = 0; c < 4; ++c) acc[q][c] = mfma16(wq[t % (DQ + 2)][ks][q], xf[ks][c], acc[q][c]);
    u32x4 u = __builtin_bit_cast(u32x4, xf[ks][0]);
    if constexpr (t == 0 && ks == 0) {
      if (g == 0) u[0] &= 0xffffu;
    }
    const bf16x8 v = __builtin_bit_cast(bf16x8, u);
    dg = mfma16(v, v, dg);
  };
  using I0 = std::integral_constant<int, 0>;
  using I1 = std::integral_constant<int, 1>;
  TS(1);
  sfor<DQ>(qissue);
  sfor<T>([&](auto tc) {
    constexpr int t = decltype(tc)::value;
    using L = std::integral_constant<int, (t + DQ < T ? t + DQ : T - 1)>;
    lgkm_bar();
    TS(8 + t);
    rd(tc, I0{});
    if constexpr (t + DQ < T) qissue(std::integral_constant<int, t + DQ>{});
    __builtin_amdgcn_sched_barrier(0);
    if constexpr (t >= 1) mm(std::integral_constant<int, t - 1>{}, I1{}, L{});
    __builtin_amdgcn_sched_barrier(0);
    rd(tc, I1{});
    __builtin_amdgcn_sched_barrier(0);
    mm(tc, I0{}, L{});
    __builtin_amdgcn_sched_barrier(0);
  });
  mm(std::integral_constant<int, T - 1>{}, I1{}, std::integral_constant<int, T - 1>{});
  TS(5);
  const unsigned ex = lds0 + ((T - 2) % NS) * 8192;
  const int q4 = i >> 2, p4 = i & 3;
  if (g == q4) {
    const float d0 = dg[0], d1 = dg[1], d2 = dg[2], d3 = dg[3];
    *(LAS float*)(uintptr_t)(ex + (w * 16 + i) * 4) = p4 == 0 ? d0 : p4 == 1 ? d1 : p4 == 2 ? d2 : d3;
  }
  lgkm_bar();
#pragma unroll
  for (int c = 0; c < 4; ++c) {
    const float s = *(const LAS float*)(uintptr_t)(ex + ((c ^ w) * 16 + i) * 4);
    const float rstd = rsqrtf(s * (1.f / D_) + EPS_);
    u32x2 hv[3];
#pragma unroll
    for (int q = 0; q < 3; ++q) {
      f32x4 v = acc[q][c] * rstd, o;
#pragma unroll
      for (int r = 0; r < 4; ++r) o[r] = v[r] * __builtin_amdgcn_rcpf(1.f + __builtin_amdgcn_exp2f(v[r] * -1.4426950408889634f));
      hv[q] = pack4(o);
    }
    char* hb = (char*)H + ((size_t)((mt * 4 + (c ^ w)) * 48 + ft * 6 + (w >> 1) * 3) * 64 + lane) * 16;
    if ((w & 1) == 0) {
      wst(hb, u32x4{hv[0][0], hv[0][1], hv[1][0], hv[1][1]});
      wst(hb + 1024, hv[2]);
    } else {
      wst(hb + 1024 + 8, hv[0]);
      wst(hb + 2048, u32x4{hv[1][0], hv[1][1], hv[2][0], hv[2][1]});
    }
  }
  TSOUT;
}

#define DQ4_ 3
constexpr int K4_NS = 8, DQ4 = DQ4_;
__global__ __launch_bounds__(512) void k4_gemm2(const u16* __restrict__ H, const u16* __restrict__ W2F, const u16* __restrict__ X1T,
                                               u16* __restrict__ X2T, float* __restrict__ part DBGARG) {
  TSDECL; TS(0);
  extern __shared__ __attribute__((aligned(16))) char smem[];
  const unsigned lds0 = (unsigned)(uintptr_t)(LAS char*)smem;
  const int tid = threadIdx.x, lane = tid & 63, wave = uni(tid >> 6), w = wave & 3, g = lane >> 4, i = lane & 15;
  const int b = blockIdx.x, xcd = b & 7, j = b >> 3;
  const int mt = (xcd >> 1) * 8 + (j & 7), nt = (xcd & 1) * 4 + (j >> 3);
  f32x4 acc[5] = {};
  u32x2 res[5];
  if (wave < 4) {
#pragma unroll
    for (int q = 0; q < 5; ++q) res[q] = *(const u32x2*)(X1T + (size_t)(nt * 80 + q * 16 + i) * SP_ + mt * 64 + w * 16 + g * 4);
  }
  if (!gemm64<5, K4_NS, DQ4, 24, 1, 0, 0, true>(acc, lds0, wave, lane, (const char*)H + (size_t)(mt * 4 + w) * 49152, lane * 16, 0, 2048,
                                                  (const char*)W2F + (size_t)nt * 5 * 49152, 49152, nullptr, 0, 0, 0, nullptr, 0 TSPASS))
    return;
  TS(5);
  f32x4 sq = {};
#pragma unroll
  for (int q = 0; q < 5; ++q) {
    const int kk = nt * 80 + q * 16 + i;
    const size_t o = (size_t)kk * SP_ + mt * 64 + w * 16 + g * 4;
    const f32x4 v = unpack4(res[q]) + acc[q];
    wst(X2T + o, pack4(v));
    if (kk != 1) sq += v * v;
  }
#pragma unroll
  for (int r = 0; r < 4; ++r) sq[r] = row16_sum(sq[r]);
  if (i == 0) wst(part + nt * S_ + mt * 64 + w * 16 + g * 4, sq);
  TSOUT;
}

__global__ __launch_bounds__(512) void k7_out(const u16* __restrict__ H, const u16* __restrict__ W2P, const u16* __restrict__ X3T,
                                             u16* __restrict__ out DBGARG) {
  TSDECL; TS(0);
  extern __shared__ __attribute__((aligned(16))) char smem[];
  const unsigned lds0 = (unsigned)(uintptr_t)(LAS char*)smem;
  const int tid = threadIdx.x, lane = tid & 63, wave = uni(tid >> 6), w = wave & 3, g = lane >> 4, i = lane & 15;
  const int b = blockIdx.x, xcd = b & 7, j = b >> 3;
  const int mt = (xcd >> 1) * 8 + (j & 7), nt = (xcd & 1) * 4 + (j >> 3);
  f32x4 acc[5] = {};
  unsigned v0, v1;
  poff_kmajor(w, lane, SP_ * 2, v0, v1);
  const size_t ro = (size_t)row_of(mt * 64 + w * 16 + i) * D_ + nt * 80 + g * 4;
  if (!gemm64<5, K4_NS, DQ4, 10, 2, 24, 1, false>(acc, lds0, wave, lane, (const char*)X3T + mt * 128, v0, v1, 64 * SP_ * 2,
                                                    (const char*)&g_FT + (size_t)nt * 5 * 20480, 20480,
                                                    (const char*)H + (size_t)(mt * 4 + w) * 49152, lane * 16, 0, 2048,
                                                    (const char*)W2P + (size_t)nt * 5 * 49152, 49152 TSPASS))
    return;
  TS(5);
#pragma unroll
  for (int q = 0; q < 5; ++q) wst(out + ro + q * 16, pack4(acc[q]));
  TSOUT;
}

extern "C" void kernel_launch(void* const* d_in, const int* in_sizes, int n_in, void* d_out, int out_size, void* d_ws, size_t ws_size,
                              hipStream_t stream) {
  const u16* x = (const u16*)d_in[0];
  const u16* wr = (const u16*)d_in[1];
  const u16* wi = (const u16*)d_in[2];
  const u16* w1 = (const u16*)d_in[4];
  const u16* w2 = (const u16*)d_in[5];
  char* ws = (char*)d_ws;
  constexpr size_t XB = (size_t)SP_ * D_ * 2;
  u16* X0T = (u16*)(ws + 0 * XB);
  u16* X1T = (u16*)(ws + 1 * XB);
  u16* X2T = (u16*)(ws + 2 * XB);
  u16* X3T = (u16*)(ws + 3 * XB);
  unsigned* WT = (unsigned*)(ws + 5 * XB);
  u16* W1F = (u16*)(ws + 6 * XB);
  u16* W2F = (u16*)(ws + 7 * XB);
  u16* W2P = (u16*)(ws + 8 * XB);
  u16* H = (u16*)(ws + 9 * XB);
  float* part0 = (float*)(ws + 12 * XB);
  float* part = part0 + 8 * S_;

  (void)hipFuncSetAttribute((const void*)k3_gemm1, hipFuncAttributeMaxDynamicSharedMemorySize, G1_NS * 8192);
  (void)hipFuncSetAttribute((const void*)k4_gemm2, hipFuncAttributeMaxDynamicSharedMemorySize, K4_NS * 8192);
  (void)hipFuncSetAttribute((const void*)k7_out, hipFuncAttributeMaxDynamicSharedMemorySize, K4_NS * 8192);

  k1_prep<<<K1_X + K1_WT, 512, 32 * RP, stream>>>(x, wr, wi, X0T, part0, WT DBGPASS(0));
  k2_cols<false><<<160 + 96, 512, C_LDS, stream>>>(X0T, part0, WT, X1T, w1, W1F, w2, W2F DBGPASS(1));
  k3_gemm1<<<256, 512, G1_NS * 8192, stream>>>(X1T, W1F, H DBGPASS(2));
  k4_gemm2<<<256, 512, K4_NS * 8192, stream>>>(H, W2F, X1T, X2T, part DBGPASS(3));
  k2_cols<true><<<160 + 48, 512, C_LDS, stream>>>(X2T, part, WT, X3T, w2, W2P, w2, W2P DBGPASS(4));
  k3_gemm1<<<256, 512, G1_NS * 8192, stream>>>(X3T, W1F, H DBGPASS(5));
  k7_out<<<256, 512, K4_NS * 8192, stream>>>(H, W2P, X3T, (u16*)d_out DBGPASS(6));
}
```

```cpp
#include <hip/hip_runtime.h>
#include <hip/hip_bf16.h>
#include <stdint.h>
#include <utility>

typedef __bf16 bf16x8 __attribute__((ext_vector_type(8)));
typedef __bf16 bf16x4 __attribute__((ext_vector_type(4)));
typedef __bf16 bf16x2 __attribute__((ext_vector_type(2)));
typedef short s16x4 __attribute__((ext_vector_type(4)));
typedef short s16x8 __attribute__((ext_vector_type(8)));
typedef float f32x4 __attribute__((ext_vector_type(4)));
typedef float f32x2 __attribute__((ext_vector_type(2)));
typedef unsigned u32x4 __attribute__((ext_vector_type(4)));
typedef unsigned u32x2 __attribute__((ext_vector_type(2)));
typedef unsigned short u16;

#define DI __device__ __forceinline__
#define LAS __attribute__((address_space(3)))

#define DBGARG
#define TSDECL
#define TSREF
#define TSPASS
#define TS(I)
#define TSOUT
#define DBGPASS(K)
constexpr int S_ = 2048, D_ = 640, FF_ = 1536;
constexpr int XP = 161 * 128;
constexpr float EPS_ = 1e-5f;

namespace tab {
constexpr double kPi = 3.14159265358979323846264338327950288;
constexpr double ccos(double x) {
  double x2 = x * x, term = 1.0, sum = 1.0;
  for (int n = 1; n <= 24; ++n) { term *= -x2 / (double)((2 * n - 1) * (2 * n)); sum += term; }
  return sum;
}
constexpr uint16_t tobf(double v) {
  float f = (float)v;
  uint32_t u = __builtin_bit_cast(uint32_t, f);
  u += 0x7fffu + ((u >> 16) & 1u);
  return (uint16_t)(u >> 16);
}
struct CosD { double c[640]; };
constexpr CosD mkcosd() {
  CosD t{};
  for (int j = 0; j < 640; ++j) {
    int jj = j <= 320 ? j : 640 - j;
    t.c[j] = (jj == 160) ? 0.0 : ccos(2.0 * kPi * jj / 640.0);
  }
  return t;
}
constexpr CosD kCd = mkcosd();
constexpr double cs(int n) { return kCd.c[n % 640]; }
constexpr double sn(int n) { return kCd.c[(n + 480) % 640]; }
constexpr double kS2 = 0.05590169943749474241, kS1 = 0.03952847075210474165;
struct Cos { uint16_t c[640]; };
constexpr Cos mkcos() {
  Cos t{};
  for (int j = 0; j < 640; ++j) t.c[j] = tobf(kS2 * kCd.c[j]);
  return t;
}
constexpr Cos kCos = mkcos();
constexpr uint16_t kR = tobf(kS1);
constexpr int bin_of(int cp) {
  int G = cp >> 4, t = (cp >> 3) & 1, j = cp & 7;
  return (t ? (G ? 40 - G : 20) : G) + 40 * j;
}
constexpr uint16_t fval(int d, int kk) {
  int c = bin_of(kk >> 1), p = kk & 1;
  if (c == 0) return (p == 0 && (d & 1)) ? (uint16_t)(kR | 0x8000u) : kR;
  return kCos.c[(d * c + 160 * p) % 640];
}
struct Chunk { uint64_t v[20 * 64 * 2]; };
constexpr Chunk mk(int n16) {
  Chunk ch{};
  for (int k32 = 0; k32 < 20; ++k32)
    for (int l = 0; l < 64; ++l) {
      int n = n16 * 16 + (l & 15), k0 = k32 * 32 + (l >> 4) * 4;
      for (int h = 0; h < 2; ++h) {
        uint64_t w = 0;
        for (int j = 0; j < 4; ++j) w |= (uint64_t)fval(n, k0 + h * 16 + j) << (16 * j);
        ch.v[(k32 * 64 + l) * 2 + h] = w;
      }
    }
  return ch;
}
template <int I> constexpr Chunk cFT = mk(I);
struct Tab { Chunk c[40]; };
template <int... I> constexpr Tab aggT(std::integer_sequence<int, I...>) { return Tab{{cFT<I>...}}; }

constexpr double aval(int r, int d1) {
  if (r >= 40 || d1 >= 40) return 0.0;
  int G = r >> 1, p = r & 1;
  if (G == 0) return (p && (d1 & 1)) ? -0.25 : 0.25;
  return p ? -0.25 * sn(16 * d1 * G) : 0.25 * cs(16 * d1 * G);
}
constexpr double cval(int G, int t, int o, int d2, int p) {
  int j = o >> 1, po = o & 1, c = bin_of(16 * G + 8 * t + j);
  if (G == 0) {
    if (p != t) return 0.0;
    if (c == 0) return (po == 0 && (d2 & 1)) ? -4.0 * kS1 : 4.0 * kS1;
    return 4.0 * kS2 * (po ? -sn(d2 * c) : cs(d2 * c));
  }
  double v = po == 0 ? (p == 0 ? cs(d2 * c) : sn(d2 * c)) : (p == 0 ? -sn(d2 * c) : cs(d2 * c));
  if (t && p) v = -v;
  return 4.0 * kS2 * v;
}
struct Fft { uint16_t c[40][64][8]; uint16_t a[6][64][8]; };
constexpr Fft mkfft() {
  Fft f{};
  for (int l = 0; l < 64; ++l) {
    int g = l >> 4, i = l & 15;
    for (int u = 0; u < 40; ++u)
      for (int e = 0; e < 8; ++e) f.c[u][l][e] = tobf(cval(u >> 1, u & 1, i, 4 * g + (e >> 1), e & 1));
    for (int T = 0; T < 3; ++T) {
      for (int e = 0; e < 8; ++e) f.a[T][l][e] = tobf(aval(16 * T + i, 16 * (e >> 2) + 4 * g + (e & 3)));
      for (int e = 4; e < 8; ++e) f.a[3 + T][l][e] = tobf(aval(16 * T + i, 28 + 4 * g + e));
    }
  }
  return f;
}
}
#if defined(__HIP_DEVICE_COMPILE__)
static __device__ const __attribute__((aligned(1024))) tab::Tab g_FT = tab::aggT(std::make_integer_sequence<int, 40>{});
static __device__ const __attribute__((aligned(1024))) tab::Fft g_FFT = tab::mkfft();
#else
static __device__ const __attribute__((aligned(1024))) tab::Tab g_FT = {};
static __device__ const __attribute__((aligned(1024))) tab::Fft g_FFT = {};
#endif

template <int... I, class F> DI void sfor_impl(std::integer_sequence<int, I...>, F&& f) {
  (f(std::integral_constant<int, I>{}), ...);
}
template <int N, class F> DI void sfor(F&& f) { sfor_impl(std::make_integer_sequence<int, N>{}, f); }

DI f32x4 mfma16(bf16x8 a, bf16x8 b, f32x4 c) { return __builtin_amdgcn_mfma_f32_16x16x32_bf16(a, b, c, 0, 0, 0); }

template <int OFF> DI void glds16(const void* sbase, unsigned voff, unsigned lds_dst) {
  unsigned keep;
  asm volatile("s_mov_b32 %0, m0\n\ts_mov_b32 m0, %3\n\ts_nop 0\n\tglobal_load_lds_dwordx4 %1, %2 offset:%4\n\ts_mov_b32 m0, %0"
               : "=&s"(keep) : "v"(voff), "s"(sbase), "s"(lds_dst), "n"(OFF) : "memory");
}
#define PRE_ 3
constexpr int last_issued(int b, int NS, int T) {
  int l = (PRE_ < NS ? PRE_ : NS) - 1;
  for (int k = 0; k < b; ++k) {
    int n = l + 2;
    if (n > k - 1 + NS) n = k - 1 + NS;
    if (n > l) l = n;
  }
  return l < T - 1 ? l : T - 1;
}
template <int N> DI void wait_bar() {
  asm volatile("s_waitcnt vmcnt(%0) lgkmcnt(0)\n\ts_barrier" ::"n"(N) : "memory");
}
DI void lgkm_bar() { asm volatile("s_waitcnt lgkmcnt(0)\n\ts_barrier" ::: "memory"); }
DI bf16x8 lds128(unsigned addr) { return *(const LAS bf16x8*)(uintptr_t)addr; }
DI bf16x8 lds_tr(unsigned a0, unsigned a1) {
  s16x4 lo = __builtin_amdgcn_ds_read_tr16_b64_v4i16((LAS s16x4*)(uintptr_t)a0);
  s16x4 hi = __builtin_amdgcn_ds_read_tr16_b64_v4i16((LAS s16x4*)(uintptr_t)a1);
  s16x8 v = __builtin_shufflevector(lo, hi, 0, 1, 2, 3, 4, 5, 6, 7);
  return __builtin_bit_cast(bf16x8, v);
}
DI unsigned pack2(float a, float b) {
  f32x2 f = {a, b};
  return __builtin_bit_cast(unsigned, __builtin_convertvector(f, bf16x2));
}
DI u32x2 pack4(f32x4 v) { return u32x2{pack2(v[0], v[1]), pack2(v[2], v[3])}; }
DI bf16x8 pack8(f32x4 a, f32x4 b) {
  u32x4 u = {pack2(a[0], a[1]), pack2(a[2], a[3]), pack2(b[0], b[1]), pack2(b[2], b[3])};
  return __builtin_bit_cast(bf16x8, u);
}
DI float bflo(unsigned u) { return __builtin_bit_cast(float, u << 16); }
DI float bfhi(unsigned u) { return __builtin_bit_cast(float, u & 0xffff0000u); }
DI f32x4 unpack4(u32x2 u) { return f32x4{bflo(u[0]), bfhi(u[0]), bflo(u[1]), bfhi(u[1])}; }
DI bf16x8 neg8(bf16x8 v) {
  u32x4 u = __builtin_bit_cast(u32x4, v);
  u ^= 0x80008000u;
  return __builtin_bit_cast(bf16x8, u);
}
DI int row_of(int m) { return 32 * (m & 63) + (m >> 6); }
DI int uni(int v) { return __builtin_amdgcn_readfirstlane(v); }
template <class T> DI void wst(void* p, T v) {
  static_assert(sizeof(T) == 16 || sizeof(T) == 8 || sizeof(T) == 4, "");
  if constexpr (sizeof(T) == 16) asm volatile("global_store_dwordx4 %0, %1, off sc1\n\ts_nop 1" ::"v"(p), "v"(v) : "memory");
  else if constexpr (sizeof(T) == 8) asm volatile("global_store_dwordx2 %0, %1, off sc1" ::"v"(p), "v"(v) : "memory");
  else asm volatile("global_store_dword %0, %1, off sc1" ::"v"(p), "v"(v) : "memory");
}

template <int OFF> DI void gld16(bf16x8& dst, const void* sbase, unsigned voff) {
  asm volatile("global_load_dwordx4 %0, %1, %2 offset:%3" : "=v"(dst) : "v"(voff), "s"(sbase), "n"(OFF) : "memory");
}
template <int N> DI void qwait(bf16x8& a, bf16x8& b) { asm volatile("s_waitcnt vmcnt(%2)" : "+v"(a), "+v"(b) : "n"(N) : "memory"); }
template <int N> DI void qwait(bf16x8& a, bf16x8& b, bf16x8& c) {
  asm volatile("s_waitcnt vmcnt(%3)" : "+v"(a), "+v"(b), "+v"(c) : "n"(N) : "memory");
}
template <int N, int NT> DI void qwaitv(bf16x8 (&f)[NT]) {
  if constexpr (NT == 2) qwait<N>(f[0], f[1]); else qwait<N>(f[0], f[1], f[2]);
}

template <int NS, int T1, int PK1, int T2, int PK2>
DI void p_loader(unsigned lds0, int w, int lane, const char* p1base, unsigned p1v0, unsigned p1v1, unsigned p1step,
                 const char* p2base, unsigned p2v0, unsigned p2v1, unsigned p2step) {
  constexpr int T = T1 + T2;
  auto issue = [&](auto tc) {
    constexpr int t = decltype(tc)::value;
    const unsigned slot = lds0 + (t % NS) * 8192;
    constexpr bool ph1 = t < T1;
    constexpr int tt = ph1 ? t : t - T1;
    constexpr int PK = ph1 ? PK1 : PK2;
    const char* pb = (ph1 ? p1base : p2base) + (size_t)tt * (ph1 ? p1step : p2step);
    if constexpr (PK == 1) {
      glds16<0>(pb, ph1 ? p1v0 : p2v0, slot + w * 2048);
      glds16<1024>(pb, ph1 ? p1v0 : p2v0, slot + w * 2048);
    } else {
      glds16<0>(pb, ph1 ? p1v0 : p2v0, slot + w * 2048);
      glds16<0>(pb, ph1 ? p1v1 : p2v1, slot + w * 2048 + 1024);
    }
  };
  sfor<last_issued(0, NS, T) + 1>(issue);
  sfor<T>([&](auto tc) {
    constexpr int b = decltype(tc)::value;
    constexpr int last = last_issued(b, NS, T), nxt = last_issued(b + 1, NS, T);
    wait_bar<2 * (last - b)>();
    sfor<nxt - last>([&](auto kc) { issue(std::integral_constant<int, last + 1 + decltype(kc)::value>{}); });
  });
}
constexpr int RP = 1296;
template <int OFF, class T> DI void gldv(T& dst, const void* sbase, unsigned voff) {
  asm volatile("global_load_dwordx4 %0, %1, %2 offset:%3" : "=v"(dst) : "v"(voff), "s"(sbase), "n"(OFF) : "memory");
}
typedef short s16x12 __attribute__((ext_vector_type(12)));
DI s16x4 lds_tr4(unsigned a) { return __builtin_amdgcn_ds_read_tr16_b64_v4i16((LAS s16x4*)(uintptr_t)a); }
template <int CTRL> DI float dpp_add(float v) {
  return v + __builtin_bit_cast(float, __builtin_amdgcn_update_dpp(0, __builtin_bit_cast(int, v), CTRL, 0xf, 0xf, true));
}
DI float row16_sum(float v) {
  return dpp_add<0x121>(dpp_add<0x122>(dpp_add<0x124>(dpp_add<0x128>(v))));
}
DI void fft_step_a(f32x4 (&a)[3], const bf16x8 (&ta)[6], unsigned a0, unsigned a1, unsigned a2) {
  const s16x4 z0 = lds_tr4(a0), z1 = lds_tr4(a1), z2 = lds_tr4(a2);
  const bf16x8 lo = __builtin_bit_cast(bf16x8, __builtin_shufflevector(z0, z1, 0, 1, 2, 3, 4, 5, 6, 7));
  const bf16x8 hi = __builtin_bit_cast(bf16x8, __builtin_shufflevector(z1, z2, 0, 1, 2, 3, 4, 5, 6, 7));
#pragma unroll
  for (int T = 0; T < 3; ++T) a[T] = mfma16(ta[T], lo, f32x4{});
#pragma unroll
  for (int T = 0; T < 3; ++T) a[T] = mfma16(ta[3 + T], hi, a[T]);
}

template <int MODE>
DI void fft640_block(int blk, unsigned lds0, const u16* __restrict__ src, u16* __restrict__ dst, float* __restrict__ part TSREF) {
  const int lane = threadIdx.x & 63, w = uni(threadIdx.x >> 6), g = lane >> 4, i = lane & 15;
  const unsigned lane16 = lane * 16;
  if constexpr (MODE != 2) {
#pragma unroll
    for (int r = 0; r < 4; ++r) {
      const int rho = 4 * w + r;
      const int grow = MODE == 0 ? row_of(blk * 32 + rho) : blk * 32 + rho;
      glds16<0>((const char*)src + (size_t)grow * 1280, lane16, lds0 + rho * RP);
    }
    if (lane < 16) {
#pragma unroll
      for (int r = 0; r < 4; ++r) {
        const int rho = 4 * w + r;
        const int grow = MODE == 0 ? row_of(blk * 32 + rho) : blk * 32 + rho;
        glds16<1024>((const char*)src + (size_t)grow * 1280, lane16, lds0 + rho * RP);
      }
    }
  } else {
    const unsigned vo = (lane >> 2) * (FF_ * 2) + (lane & 3) * 16;
#pragma unroll
    for (int p = 0; p < 5; ++p)
      glds16<0>((const char*)src + blk * 64 + (size_t)(5 * w + p) * (16 * FF_ * 2), vo, lds0 + (5 * w + p) * 1088);
  }
  if constexpr (MODE == 3) {
    wait_bar<0>();
    const unsigned rb = lds0 + (4 * g + (i >> 2)) * RP + (i & 3) * 8 + w * 160;
#pragma unroll
    for (int q = 0; q < 5; ++q)
      wst((char*)dst + ((size_t)((5 * w + q) * 48 + blk) * 64 + lane) * 16, lds_tr(rb + q * 32, rb + q * 32 + 16 * RP));
    return;
  }
  const char* tb = (const char*)&g_FFT;
  bf16x8 ta[6], tc[5];
  sfor<6>([&](auto uc) { constexpr int u = decltype(uc)::value; gld16<(u & 3) * 1024>(ta[u], tb + 40960 + (u >> 2) * 4096, lane16); });
  sfor<5>([&](auto uc) { constexpr int u = decltype(uc)::value; gld16<(u & 3) * 1024>(tc[u], tb + w * 5120 + (u >> 2) * 4096, lane16); });
  asm volatile("s_waitcnt vmcnt(5)" : "+v"(ta[0]), "+v"(ta[1]), "+v"(ta[2]), "+v"(ta[3]), "+v"(ta[4]), "+v"(ta[5])::"memory");
  if constexpr (MODE == 2) lgkm_bar();
  TS(1);
  if constexpr (MODE < 2) {
    f32x4 a[4][3];
#pragma unroll
    for (int r = 0; r < 4; ++r) {
      const unsigned rb = lds0 + (4 * w + r) * RP;
      fft_step_a(a[r], ta, rb + lane * 8, rb + 512 + lane * 8, rb + 1024 + (lane & 31) * 8);
    }
    const unsigned wb = lds0 + 4 * w * RP + g * 128 + i * 4;
#pragma unroll
    for (int r = 0; r < 4; ++r)
#pragma unroll
      for (int T = 0; T < 2; ++T) {
        *(LAS unsigned*)(uintptr_t)(wb + r * RP + T * 512) = pack2(a[r][T][0], a[r][T][1]);
        *(LAS unsigned*)(uintptr_t)(wb + r * RP + T * 512 + 64) = pack2(a[r][T][2], a[r][T][3]);
      }
    if (g < 2) {
#pragma unroll
      for (int r = 0; r < 4; ++r) {
        *(LAS unsigned*)(uintptr_t)(wb + r * RP + 1024) = pack2(a[r][2][0], a[r][2][1]);
        *(LAS unsigned*)(uintptr_t)(wb + r * RP + 1024 + 64) = pack2(a[r][2][2], a[r][2][3]);
      }
    }
  } else {
    const int j = w & 3, ft = w >> 2;
    const unsigned rb = lds0 + (4 * g + (i >> 2)) * 1088 + j * 256 + (i & 3) * 8 + ft * 32;
    const unsigned rt = lds0 + (32 + 4 * (g & 1) + (i >> 2)) * 1088 + j * 256 + (i & 3) * 8 + ft * 32;
    f32x4 a[4][3];
#pragma unroll
    for (int dd = 0; dd < 4; ++dd) fft_step_a(a[dd], ta, rb + dd * 64, rb + dd * 64 + 16 * 1088, rt + dd * 64);
    u32x4 u[3][2];
#pragma unroll
    for (int T = 0; T < 3; ++T)
#pragma unroll
      for (int dd = 0; dd < 4; ++dd) {
        u[T][0][dd] = pack2(a[dd][T][0], a[dd][T][1]);
        u[T][1][dd] = pack2(a[dd][T][2], a[dd][T][3]);
      }
    lgkm_bar();
    const unsigned wb = lds0 + (16 * ft + i) * RP + g * 128 + j * 16;
#pragma unroll
    for (int T = 0; T < 3; ++T)
      if (T < 2 || g < 2) {
        *(LAS u32x4*)(uintptr_t)(wb + T * 512) = u[T][0];
        *(LAS u32x4*)(uintptr_t)(wb + T * 512 + 64) = u[T][1];
      }
  }
  TS(2);
  lgkm_bar();
  asm volatile("s_waitcnt vmcnt(0)" : "+v"(tc[0]), "+v"(tc[1]), "+v"(tc[2]), "+v"(tc[3]), "+v"(tc[4])::"memory");
  TS(3);
  const unsigned db = lds0 + i * RP + g * 16;
  float sq[2] = {};
#pragma unroll
  for (int q = 0; q < 5; ++q) {
    const int u = 5 * w + q, G = u >> 1;
    const bf16x8 d0 = lds128(db + G * 64), d1 = lds128(db + 16 * RP + G * 64);
    if constexpr (MODE == 1) {
      const f32x4 a = mfma16(d0, tc[q], f32x4{}), b = mfma16(d1, tc[q], f32x4{});
      wst((char*)dst + ((size_t)(u * 48 + blk) * 64 + lane) * 16, pack8(a, b));
    } else {
      f32x4 a = mfma16(tc[q], d0, f32x4{}), b = mfma16(tc[q], d1, f32x4{});
      if constexpr (MODE == 0) {
        char* o = (char*)dst + (size_t)(2 * blk) * XP + u * 512 + lane * 8;
        wst(o, pack4(a));
        wst(o + XP, pack4(b));
      }
      if (q == 0 && w == 0 && g == 0) { a[1] = 0.f; b[1] = 0.f; }
      if constexpr (MODE == 0) {
        sq[0] += (a[0] * a[0] + a[1] * a[1]) + (a[2] * a[2] + a[3] * a[3]);
        sq[1] += (b[0] * b[0] + b[1] * b[1]) + (b[2] * b[2] + b[3] * b[3]);
      } else {
        char* o = (char*)dst + ((size_t)(2 * blk * 20 + G) * 64 + lane) * 16 + (u & 1) * 8;
        wst(o, pack4(a));
        wst(o + 20480, pack4(b));
      }
    }
  }
  if constexpr (MODE == 0) {
    float s = (g & 1) ? sq[1] : sq[0];
    s += __shfl_xor((g & 1) ? sq[0] : sq[1], 16);
    s += __shfl_xor(s, 32);
    if (g < 2) wst(part + w * S_ + blk * 32 + lane, s);
  }
}

DI void prefetch32k(const void* p, int blk) {
  u32x4 v[4];
#pragma unroll
  for (int q = 0; q < 4; ++q) gldv<0>(v[q], (const char*)p + (size_t)blk * 32768 + q * 8192, threadIdx.x * 16);
  asm volatile("s_waitcnt vmcnt(0)" : "+v"(v[0]), "+v"(v[1]), "+v"(v[2]), "+v"(v[3])::"memory");
}
#define K1_PF 120
constexpr int K1_X = 64, K1_WT = 160;
__global__ __launch_bounds__(512) void k1_prep(const u16* __restrict__ x, const u16* __restrict__ wr, const u16* __restrict__ wi,
                                              const u16* __restrict__ w1, const u16* __restrict__ w2, u16* __restrict__ X0T,
                                              float* __restrict__ part, unsigned* __restrict__ WT DBGARG) {
  TSDECL; TS(0);
  extern __shared__ __attribute__((aligned(16))) char smem[];
  const unsigned lds0 = (unsigned)(uintptr_t)(LAS char*)smem;
  const int tid = threadIdx.x;
  int b = blockIdx.x;
  if (b < K1_X) {
    fft640_block<0>(b, lds0, x, X0T, part TSPASS);
  } else if (b >= K1_X + K1_WT) {
    b -= K1_X + K1_WT;
    prefetch32k(b < 60 ? w1 : w2, b < 60 ? b : b - 60);
  } else {
    b -= K1_X;
    const int kt = b % 10, ot = b / 10;
    const int kc = tid & 31, r = tid >> 5, e = r & 7;
    const int t0 = 16 * (ot >> 2) + (r >> 3) + 64 * (16 * (e >> 2) + 4 * (ot & 3) + (e & 3));
    const u16* pr = wr + t0 * 321 + kt * 32 + kc + 1;
    const u16* pi = wi + t0 * 321 + kt * 32 + kc + 1;
    unsigned v[8];
#pragma unroll
    for (int it = 0; it < 8; ++it) v[it] = (unsigned)pr[it * 642] | ((unsigned)pi[it * 642] << 16);
    const unsigned wa = lds0 + (kc * 132 + r) * 4;
#pragma unroll
    for (int it = 0; it < 8; ++it) *(LAS unsigned*)(uintptr_t)(wa + it * 64) = v[it];
    lgkm_bar();
#pragma unroll
    for (int it = 0; it < 2; ++it) {
      const int kc2 = r + 16 * it;
      const int c = (kt * 32 + kc2 + 1) % 320, rr = c % 40, j = c / 40;
      const int cp = (rr == 0 ? 0 : rr == 20 ? 8 : rr < 20 ? 16 * rr : 16 * (40 - rr) + 8) + j;
      wst(WT + cp * 2048 + ot * 128 + kc * 4, *(const LAS u32x4*)(uintptr_t)(lds0 + (kc2 * 132 + kc * 4) * 4));
    }
  }
  TSOUT;
}

struct Cx { float c, s; };
DI Cx cis(float rev) { return Cx{__builtin_amdgcn_cosf(rev), __builtin_amdgcn_sinf(rev)}; }
DI Cx cmul(Cx a, Cx b) { return Cx{a.c * b.c - a.s * b.s, a.c * b.s + a.s * b.c}; }
constexpr int C_CH = 576, C_PL = 8 * C_CH, C_OP = 32 * 272;
constexpr int C_MID = 2 * C_OP, C_HALF = C_MID + 2 * C_PL, C_LDS = 2 * C_HALF;
#define PDELAY 0
template <bool SECOND>
__global__ __launch_bounds__(512) void k2_cols(const u16* Xin, const float* __restrict__ part, const unsigned* __restrict__ WT,
                                              u16* Xout, const u16* __restrict__ wa, u16* __restrict__ Wa,
                                              const u16* __restrict__ wb, u16* __restrict__ Wb DBGARG) {
  TSDECL; TS(0);
  extern __shared__ __attribute__((aligned(16))) char smem[];
  if (blockIdx.x >= 160) {
    const unsigned l0 = (unsigned)(uintptr_t)(LAS char*)smem;
    const int b = blockIdx.x - 160;
    if (PDELAY) __builtin_amdgcn_s_sleep(PDELAY);
    if constexpr (SECOND) {
      if (b < 48) fft640_block<3>(b, l0, wa, Wa, nullptr TSPASS);
      else prefetch32k(&g_FT, b - 48);
    }
    else if (b < 48) fft640_block<2>(b, l0, wa, Wa, nullptr TSPASS);
    else fft640_block<1>(b - 48, l0, wb, Wb, nullptr TSPASS);
    TSOUT;
    return;
  }
  const int half = uni(threadIdx.x >> 8), c = 2 * blockIdx.x + half;
  const unsigned ldsb = (unsigned)(uintptr_t)(LAS char*)smem, lds0 = ldsb + half * C_HALF;
  const int tid = threadIdx.x & 255, lane = tid & 63, w = uni(tid >> 6), g = lane >> 4, i = lane & 15;
  const int T = threadIdx.x;
  f32x4 ps[8];
  u32x4 xin[2], wv[2], res[2];
  const char* xb = (const char*)Xin + blockIdx.x * 128;
  const unsigned vin = (T >> 2) * XP + (T & 3) * 32, vout = (T >> 3) * XP + (T & 7) * 16;
  gldv<0>(xin[0], xb, vin);
  gldv<16>(xin[1], xb, vin);
#pragma unroll
  for (int p = 0; p < 8; ++p) gldv<0>(ps[p], part + p * S_, T * 16);
  gldv<0>(wv[0], WT + (size_t)c * 2048, tid * 32);
  gldv<16>(wv[1], WT + (size_t)c * 2048, tid * 32);
  gldv<0>(res[0], xb, vout);
  gldv<0>(res[1], xb + 64 * XP, vout);
  __builtin_amdgcn_sched_barrier(0);
  bf16x8 f1r[2], f1i[2], f1n[2], f2r[2], f2i[2], f2n[2];
  const int k1c = 16 * w + i;
  const unsigned odd = (i & 1) ? 0x80008000u : 0u;
  {
    const Cx st = cis((float)k1c * (-1.f / 64.f));
    Cx v = cis((float)((g * k1c) & 7) * (-1.f / 8.f));
    v.c *= 0.125f; v.s *= 0.125f;
    f32x4 cr[2], ci[2];
#pragma unroll
    for (int j = 0; j < 8; ++j) {
      cr[j >> 2][j & 3] = v.c; ci[j >> 2][j & 3] = v.s;
      if (j < 7) v = cmul(v, st);
    }
    f1r[0] = pack8(cr[0], cr[1]);
    f1i[0] = pack8(ci[0], ci[1]);
    f1r[1] = __builtin_bit_cast(bf16x8, __builtin_bit_cast(u32x4, f1r[0]) ^ odd);
    f1i[1] = __builtin_bit_cast(bf16x8, __builtin_bit_cast(u32x4, f1i[0]) ^ odd);
    f1n[0] = neg8(f1i[0]);
    f1n[1] = neg8(f1i[1]);
  }
  {
    const Cx st = cis((float)i * (-1.f / 32.f));
    Cx v = cis((float)((g * i) & 7) * (-1.f / 8.f));
    v.c *= 0.17677669529663688f; v.s *= 0.17677669529663688f;
    f32x4 cr, ci;
#pragma unroll
    for (int j = 0; j < 4; ++j) {
      cr[j] = v.c; ci[j] = v.s;
      if (j < 3) v = cmul(v, st);
    }
    const u32x2 pr = pack4(cr), pi = pack4(ci);
    const u32x4 r0 = {pr[0], pr[1], pr[0] ^ odd, pr[1] ^ odd}, i0 = {pi[0], pi[1], pi[0] ^ odd, pi[1] ^ odd};
    f2r[0] = __builtin_bit_cast(bf16x8, r0);
    f2i[0] = __builtin_bit_cast(bf16x8, i0);
    f2r[1] = __builtin_bit_cast(bf16x8, r0 ^ 0x80000000u);
    f2i[1] = __builtin_bit_cast(bf16x8, i0 ^ 0x80000000u);
    f2n[0] = neg8(f2i[0]);
    f2n[1] = neg8(f2i[1]);
  }
  f32x4 twc[2], tws[2], tvc[2], tvs[2];
  {
    const Cx st = cis((float)k1c * (1.f / 2048.f)), jp = cis((float)k1c * (1.f / 128.f));
    Cx v = cis((float)(g * k1c) * (1.f / 512.f));
#pragma unroll
    for (int r = 0; r < 4; ++r) {
      const Cx u = cmul(v, jp);
      twc[0][r] = v.c; tws[0][r] = v.s; twc[1][r] = u.c; tws[1][r] = u.s;
      if (r < 3) v = cmul(v, st);
    }
#pragma unroll
    for (int mt = 0; mt < 2; ++mt) {
      const Cx s2 = cis((float)(16 * mt + i) * (1.f / 2048.f));
      Cx u = cis((float)((16 * mt + i) * (4 * w + g)) * (1.f / 512.f));
#pragma unroll
      for (int r = 0; r < 4; ++r) {
        tvc[mt][r] = u.c; tvs[mt][r] = u.s;
        if (r < 3) u = cmul(u, s2);
      }
    }
  }
  asm volatile("" : "+v"(f1r[0]), "+v"(f1r[1]), "+v"(f1i[0]), "+v"(f1i[1]), "+v"(f1n[0]), "+v"(f1n[1]), "+v"(f2r[0]), "+v"(f2r[1]),
               "+v"(f2i[0]), "+v"(f2i[1]), "+v"(f2n[0]), "+v"(f2n[1]));
  asm volatile("" : "+v"(twc[0]), "+v"(twc[1]), "+v"(tws[0]), "+v"(tws[1]), "+v"(tvc[0]), "+v"(tvc[1]), "+v"(tvs[0]), "+v"(tvs[1]));
  TS(1);
  asm volatile("s_waitcnt vmcnt(4)" : "+v"(ps[0]), "+v"(ps[1]), "+v"(ps[2]), "+v"(ps[3]), "+v"(ps[4]), "+v"(ps[5]), "+v"(ps[6]), "+v"(ps[7]),
               "+v"(xin[0]), "+v"(xin[1])::"memory");
  {
    f32x4 rs = ((ps[0] + ps[1]) + (ps[2] + ps[3])) + ((ps[4] + ps[5]) + (ps[6] + ps[7]));
#pragma unroll
    for (int e = 0; e < 4; ++e) rs[e] = rsqrtf(rs[e] * (1.f / D_) + EPS_);
    const unsigned ad = ldsb + ((T & 15) >> 1) * C_CH + (T >> 4) * 16 + (T & 1) * 8;
#pragma unroll
    for (int cc = 0; cc < 2; ++cc) {
      const unsigned x0 = xin[0][cc], x1 = xin[0][2 + cc], x2 = xin[1][cc], x3 = xin[1][2 + cc];
      const u32x2 re = {pack2(bflo(x0) * rs[0], bflo(x1) * rs[1]), pack2(bflo(x2) * rs[2], bflo(x3) * rs[3])};
      u32x2 im = {pack2(bfhi(x0) * rs[0], bfhi(x1) * rs[1]), pack2(bfhi(x2) * rs[2], bfhi(x3) * rs[3])};
      if (cc == 0 && blockIdx.x == 0) im = u32x2{0, 0};
      *(LAS u32x2*)(uintptr_t)(ad + cc * C_HALF) = re;
      *(LAS u32x2*)(uintptr_t)(ad + cc * C_HALF + C_PL) = im;
    }
  }
  asm volatile("s_waitcnt vmcnt(2) lgkmcnt(0)\n\ts_barrier" : "+v"(wv[0]), "+v"(wv[1])::"memory");
  TS(2);
  const unsigned rd = lds0 + g * C_CH + i * 16;
  {
    f32x4 ar[2] = {}, ai[2] = {};
#pragma unroll
    for (int mt = 0; mt < 2; ++mt)
#pragma unroll
      for (int ks = 0; ks < 2; ++ks) {
        const bf16x8 ur = lds128(rd + ks * 4 * C_CH + mt * 256), ui = lds128(rd + C_PL + ks * 4 * C_CH + mt * 256);
        ar[mt] = mfma16(ur, f1r[ks], ar[mt]);
        ai[mt] = mfma16(ur, f1i[ks], ai[mt]);
        ar[mt] = mfma16(ui, f1n[ks], ar[mt]);
        ai[mt] = mfma16(ui, f1r[ks], ai[mt]);
      }
    f32x4 br[2], bi[2];
#pragma unroll
    for (int mt = 0; mt < 2; ++mt) {
      br[mt] = ar[mt] * twc[mt] + ai[mt] * tws[mt];
      bi[mt] = ai[mt] * twc[mt] - ar[mt] * tws[mt];
    }
    bf16x8 pr = pack8(br[0], br[1]), pi = pack8(bi[0], bi[1]);
    f32x4 yr[2] = {}, yi[2] = {};
#pragma unroll
    for (int mt = 0; mt < 2; ++mt) {
      yr[mt] = mfma16(f2r[mt], pr, yr[mt]);
      yi[mt] = mfma16(f2i[mt], pr, yi[mt]);
      yr[mt] = mfma16(f2n[mt], pi, yr[mt]);
      yi[mt] = mfma16(f2r[mt], pi, yi[mt]);
    }
#pragma unroll
    for (int mt = 0; mt < 2; ++mt) {
      const u32x4 ww = wv[mt];
#pragma unroll
      for (int r = 0; r < 4; ++r) {
        const float wr_ = bflo(ww[r]), wi_ = bfhi(ww[r]);
        br[mt][r] = yr[mt][r] * wr_ - yi[mt][r] * wi_;
        bi[mt][r] = yr[mt][r] * wi_ + yi[mt][r] * wr_;
      }
    }
    pr = pack8(br[0], br[1]);
    pi = pack8(bi[0], bi[1]);
    f32x4 zr[2] = {}, zi[2] = {};
#pragma unroll
    for (int nt = 0; nt < 2; ++nt) {
      zr[nt] = mfma16(pr, f2r[nt], zr[nt]);
      zi[nt] = mfma16(pi, f2r[nt], zi[nt]);
      zr[nt] = mfma16(pi, f2i[nt], zr[nt]);
      zi[nt] = mfma16(pr, f2n[nt], zi[nt]);
    }
    const unsigned ad = lds0 + C_MID + (2 * w + (g >> 1)) * C_CH + i * 16 + (g & 1) * 8;
#pragma unroll
    for (int nt = 0; nt < 2; ++nt) {
      const f32x4 a = zr[nt] * tvc[nt] - zi[nt] * tvs[nt];
      const f32x4 b = zi[nt] * tvc[nt] + zr[nt] * tvs[nt];
      *(LAS u32x2*)(uintptr_t)(ad + nt * 256) = pack4(a);
      *(LAS u32x2*)(uintptr_t)(ad + C_PL + nt * 256) = pack4(b);
    }
  }
  lgkm_bar();
  TS(3);
  {
    const unsigned r2 = rd + C_MID;
    const unsigned ad = lds0 + i * 272 + w * 64 + g * 16;
#pragma unroll
    for (int nt = 0; nt < 2; ++nt) {
      f32x4 vr = {}, vi = {};
#pragma unroll
      for (int ks = 0; ks < 2; ++ks) {
        const bf16x8 qr = lds128(r2 + ks * 4 * C_CH + nt * 256), qi = lds128(r2 + C_PL + ks * 4 * C_CH + nt * 256);
        vr = mfma16(f1r[ks], qr, vr);
        vi = mfma16(f1r[ks], qi, vi);
        vr = mfma16(f1i[ks], qi, vr);
        vi = mfma16(f1n[ks], qr, vi);
      }
      *(LAS f32x4*)(uintptr_t)(ad + nt * 16 * 272) = vr;
      *(LAS f32x4*)(uintptr_t)(ad + C_OP + nt * 16 * 272) = vi;
    }
  }
  asm volatile("s_waitcnt vmcnt(0) lgkmcnt(0)\n\ts_barrier" : "+v"(res[0]), "+v"(res[1])::"memory");
  TS(4);
  const unsigned ro = ldsb + (T >> 5) * 272 + (T & 31) * 8;
#pragma unroll
  for (int hh = 0; hh < 2; ++hh) {
    f32x2 v[4];
#pragma unroll
    for (int p = 0; p < 4; ++p) v[p] = *(const LAS f32x2*)(uintptr_t)(ro + hh * 16 * 272 + (p >> 1) * C_HALF + (p & 1) * C_OP);
    if (blockIdx.x == 0) v[1] = f32x2{0.f, 0.f};
    const u32x4 r = res[hh];
    const u32x4 o = {pack2(bflo(r[0]) + v[0][0], bfhi(r[0]) + v[1][0]), pack2(bflo(r[1]) + v[2][0], bfhi(r[1]) + v[3][0]),
                     pack2(bflo(r[2]) + v[0][1], bfhi(r[2]) + v[1][1]), pack2(bflo(r[3]) + v[2][1], bfhi(r[3]) + v[3][1])};
    wst((char*)Xout + blockIdx.x * 128 + hh * 64 * XP + vout, o);
  }
  TSOUT;
}

#define DQ3_ 2
DI bf16x8 lds_2x64(unsigned a) {
  const u32x2 lo = *(const LAS u32x2*)(uintptr_t)a, hi = *(const LAS u32x2*)(uintptr_t)(a + 512);
  return __builtin_bit_cast(bf16x8, u32x4{lo[0], lo[1], hi[0], hi[1]});
}
constexpr int G1_NS = 8, DQ3 = DQ3_;
__global__ __launch_bounds__(512) void k3_gemm1(const u16* __restrict__ XT, const u16* __restrict__ W1F, u16* __restrict__ H DBGARG) {
  TSDECL; TS(0);
  extern __shared__ __attribute__((aligned(16))) char smem[];
  const unsigned lds0 = (unsigned)(uintptr_t)(LAS char*)smem;
  const int tid = threadIdx.x, lane = tid & 63, wave = uni(tid >> 6), w = wave & 3, g = lane >> 4, i = lane & 15;
  const int b = blockIdx.x, xcd = b & 7, j = b >> 3;
  const int mt = (xcd >> 1) * 8 + (j & 7), ft = (xcd & 1) * 4 + (j >> 3);
  constexpr int T = 10, NS = G1_NS, DQ = DQ3;
  if (wave >= 4) {
    p_loader<NS, T, 1, 0, 0>(lds0, w, lane, (const char*)XT + (size_t)(mt * 4 + w) * XP, lane * 16, 0, 2048, nullptr, 0, 0, 0);
    return;
  }
  const char* wb = (const char*)W1F + (size_t)(ft * 12 + w * 3) * 20480;
  const unsigned lane16 = lane * 16;
  f32x4 acc[3][4] = {};
  f32x4 dg = {};
  bf16x8 xf[2][4], wq[DQ + 2][2][3];
  auto qissue = [&](auto tc) {
    constexpr int t = decltype(tc)::value;
#pragma unroll
    for (int q = 0; q < 3; ++q) gld16<0>(wq[t % (DQ + 2)][0][q], wb + q * 20480 + t * 2048, lane16);
#pragma unroll
    for (int q = 0; q < 3; ++q) gld16<1024>(wq[t % (DQ + 2)][1][q], wb + q * 20480 + t * 2048, lane16);
  };
  auto rd = [&](auto tc, auto kc) {
    constexpr int t = decltype(tc)::value, ks = decltype(kc)::value;
#pragma unroll
    for (int c = 0; c < 4; ++c) xf[ks][c] = lds_2x64(lds0 + (t % NS) * 8192 + (c ^ w) * 2048 + ks * 1024 + lane * 8);
  };
  auto mm = [&](auto tc, auto kc, auto lc) {
    constexpr int t = decltype(tc)::value, ks = decltype(kc)::value, last = decltype(lc)::value;
    qwaitv<6 * (last - t) + (ks ? 0 : 3), 3>(wq[t % (DQ + 2)][ks]);
#pragma unroll
    for (int q = 0; q < 3; ++q)
#pragma unroll
      for (int c = 0; c < 4; ++c) acc[q][c] = mfma16(wq[t % (DQ + 2)][ks][q], xf[ks][c], acc[q][c]);
    u32x4 u = __builtin_bit_cast(u32x4, xf[ks][0]);
    if constexpr (t == 0 && ks == 0) {
      if (g == 0) u[0] &= 0xffffu;
    }
    const bf16x8 v = __builtin_bit_cast(bf16x8, u);
    dg = mfma16(v, v, dg);
  };
  using I0 = std::integral_constant<int, 0>;
  using I1 = std::integral_constant<int, 1>;
  TS(1);
  sfor<DQ>(qissue);
  sfor<T>([&](auto tc) {
    constexpr int t = decltype(tc)::value;
    using L = std::integral_constant<int, (t + DQ < T ? t + DQ : T - 1)>;
    lgkm_bar();
    TS(8 + t);
    rd(tc, I0{});
    if constexpr (t + DQ < T) qissue(std::integral_constant<int, t + DQ>{});
    __builtin_amdgcn_sched_barrier(0);
    if constexpr (t >= 1) mm(std::integral_constant<int, t - 1>{}, I1{}, L{});
    __builtin_amdgcn_sched_barrier(0);
    rd(tc, I1{});
    __builtin_amdgcn_sched_barrier(0);
    mm(tc, I0{}, L{});
    __builtin_amdgcn_sched_barrier(0);
  });
  mm(std::integral_constant<int, T - 1>{}, I1{}, std::integral_constant<int, T - 1>{});
  TS(5);
  const unsigned ex = lds0 + ((T - 2) % NS) * 8192;
  const int q4 = i >> 2, p4 = i & 3;
  if (g == q4) {
    const float d0 = dg[0], d1 = dg[1], d2 = dg[2], d3 = dg[3];
    *(LAS float*)(uintptr_t)(ex + (w * 16 + i) * 4) = p4 == 0 ? d0 : p4 == 1 ? d1 : p4 == 2 ? d2 : d3;
  }
  lgkm_bar();
#pragma unroll
  for (int c = 0; c < 4; ++c) {
    const float s = *(const LAS float*)(uintptr_t)(ex + ((c ^ w) * 16 + i) * 4);
    const float rstd = rsqrtf(s * (1.f / D_) + EPS_);
    u32x2 hv[3];
#pragma unroll
    for (int q = 0; q < 3; ++q) {
      f32x4 v = acc[q][c] * rstd, o;
#pragma unroll
      for (int r = 0; r < 4; ++r) o[r] = v[r] * __builtin_amdgcn_rcpf(1.f + __builtin_amdgcn_exp2f(v[r] * -1.4426950408889634f));
      hv[q] = pack4(o);
    }
    char* hb = (char*)H + ((size_t)((mt * 4 + (c ^ w)) * 48 + ft * 6 + (w >> 1) * 3) * 64 + lane) * 16;
    if ((w & 1) == 0) {
      wst(hb, u32x4{hv[0][0], hv[0][1], hv[1][0], hv[1][1]});
      wst(hb + 1024, hv[2]);
    } else {
      wst(hb + 1024 + 8, hv[0]);
      wst(hb + 2048, u32x4{hv[1][0], hv[1][1], hv[2][0], hv[2][1]});
    }
  }
  TSOUT;
}

template <int OFF> DI void gld8(u32x2& dst, const void* sbase, unsigned voff) {
  asm volatile("global_load_dwordx2 %0, %1, %2 offset:%3" : "=v"(dst) : "v"(voff), "s"(sbase), "n"(OFF) : "memory");
}
#define G2D 4
#define G2E 5
constexpr int G2_LDS = 140 * 1024;
template <int N1, int N2>
DI void g2_stream(f32x4 (&acc)[5][4], int lane, int cx, const char* a1, const char* b1, const char* a2, const char* b2 TSREF) {
  constexpr int N = N1 + N2, D = G2D;
  bf16x8 af[D][5], bf[D][4];
  u32x2 bl[D][8];
  const unsigned l16 = lane * 16, l8 = lane * 8;
  auto issue = [&](auto sc) {
    constexpr int s = decltype(sc)::value;
    if constexpr (s < N1) {
#pragma unroll
      for (int c = 0; c < 4; ++c) {
        gld8<0>(bl[s % D][2 * c], b1 + (size_t)(c ^ cx) * XP + s * 1024, l8);
        gld8<512>(bl[s % D][2 * c + 1], b1 + (size_t)(c ^ cx) * XP + s * 1024, l8);
      }
#pragma unroll
      for (int q = 0; q < 5; ++q) gld16<0>(af[s % D][q], a1 + q * 20480 + s * 1024, l16);
    } else {
#pragma unroll
      for (int c = 0; c < 4; ++c) gld16<0>(bf[s % D][c], b2 + (size_t)(c ^ cx) * 49152 + (s - N1) * 1024, l16);
#pragma unroll
      for (int q = 0; q < 5; ++q) gld16<0>(af[s % D][q], a2 + q * 49152 + (s - N1) * 1024, l16);
    }
  };
  sfor<(D < N ? D : N)>(issue);
  sfor<N>([&](auto sc) {
    constexpr int s = decltype(sc)::value, last = (s + D - 1 < N - 1 ? s + D - 1 : N - 1);
    constexpr int n1 = (last < N1 ? last : N1 - 1) - s;
    constexpr int pend = (n1 > 0 ? n1 : 0) * 13 + ((last - s) - (n1 > 0 ? n1 : 0)) * 9;
    bf16x8(&a)[5] = af[s % D];
    if constexpr (s < N1) {
      u32x2(&l)[8] = bl[s % D];
      asm volatile("s_waitcnt vmcnt(%13)" : "+v"(a[0]), "+v"(a[1]), "+v"(a[2]), "+v"(a[3]), "+v"(a[4]), "+v"(l[0]), "+v"(l[1]), "+v"(l[2]),
                   "+v"(l[3]), "+v"(l[4]), "+v"(l[5]), "+v"(l[6]), "+v"(l[7]) : "n"(pend) : "memory");
#pragma unroll
      for (int c = 0; c < 4; ++c)
        bf[s % D][c] = __builtin_bit_cast(bf16x8, u32x4{l[2 * c][0], l[2 * c][1], l[2 * c + 1][0], l[2 * c + 1][1]});
    } else {
      bf16x8(&b)[4] = bf[s % D];
      asm volatile("s_waitcnt vmcnt(%9)" : "+v"(a[0]), "+v"(a[1]), "+v"(a[2]), "+v"(a[3]), "+v"(a[4]), "+v"(b[0]), "+v"(b[1]), "+v"(b[2]),
                   "+v"(b[3]) : "n"(pend) : "memory");
    }
    TS(8 + s);
#pragma unroll
    for (int q = 0; q < 5; ++q)
#pragma unroll
      for (int c = 0; c < 4; ++c) acc[q][c] = mfma16(a[q], bf[s % D][c], acc[q][c]);
    __builtin_amdgcn_sched_barrier(0);
    if constexpr (s + D < N) issue(std::integral_constant<int, s + D>{});
    __builtin_amdgcn_sched_barrier(0);
  });
}
template <bool LAST>
__global__ __launch_bounds__(512) void k4_gemm2(const u16* __restrict__ H, const u16* __restrict__ W2, const u16* __restrict__ X,
                                               u16* __restrict__ Y, float* __restrict__ part DBGARG) {
  TSDECL; TS(0);
  extern __shared__ __attribute__((aligned(16))) char smem[];
  const unsigned lds0 = (unsigned)(uintptr_t)(LAS char*)smem;
  const int tid = threadIdx.x, lane = tid & 63, wave = uni(tid >> 6), w = wave & 3, g = lane >> 4, i = lane & 15;
  const int b = blockIdx.x, xcd = b & 7, j = b >> 3;
  const int mt = (xcd >> 1) * 8 + (j & 7), nt = (xcd & 1) * 4 + (j >> 3);
  constexpr int E2 = LAST ? 6 : G2E, L2 = 12 - E2, E1 = LAST ? 2 : 0, L1 = LAST ? 3 : 0;
  const char* a1 = (const char*)&g_FT + (size_t)nt * 5 * 20480;
  const char* b1 = (const char*)X + (size_t)mt * 4 * XP;
  const char* a2 = (const char*)W2 + (size_t)nt * 5 * 49152;
  const char* b2 = (const char*)H + (size_t)mt * 4 * 49152;
  f32x4 acc[5][4] = {};
  if (wave >= 4) {
    g2_stream<E1, E2>(acc, lane, 0, a1 + (4 * L1 + w * E1) * 1024, b1 + (4 * L1 + w * E1) * 1024, a2 + (4 * L2 + w * E2) * 1024,
                      b2 + (4 * L2 + w * E2) * 1024 TSPASS);
    const unsigned ex = lds0 + w * 20480 + lane * 16;
#pragma unroll
    for (int c = 0; c < 4; ++c)
#pragma unroll
      for (int q = 0; q < 5; ++q) *(LAS f32x4*)(uintptr_t)(ex + (c * 5 + q) * 1024) = acc[q][c];
    lgkm_bar();
    return;
  }
  u32x2 res[5];
  if constexpr (!LAST) {
#pragma unroll
    for (int q = 0; q < 5; ++q) gld8<0>(res[q], b1 + (size_t)w * XP + (nt * 5 + q) * 512, lane * 8);
  }
  g2_stream<L1, L2>(acc, lane, w, a1 + w * L1 * 1024, b1 + w * L1 * 1024, a2 + w * L2 * 1024, b2 + w * L2 * 1024 TSPASS);
  TS(4);
  {
    const unsigned ex = lds0 + 81920 + w * 15360 + lane * 16;
#pragma unroll
    for (int c = 1; c < 4; ++c)
#pragma unroll
      for (int q = 0; q < 5; ++q) *(LAS f32x4*)(uintptr_t)(ex + ((c - 1) * 5 + q) * 1024) = acc[q][c];
  }
  f32x4 v[5];
  lgkm_bar();
#pragma unroll
  for (int q = 0; q < 5; ++q) {
    const unsigned e0 = lds0 + (w * 5 + q) * 1024 + lane * 16;
    f32x4 s0 = acc[q][0] + *(const LAS f32x4*)(uintptr_t)(e0), s1 = *(const LAS f32x4*)(uintptr_t)(e0 + 20480);
    s0 += *(const LAS f32x4*)(uintptr_t)(e0 + 40960);
    s1 += *(const LAS f32x4*)(uintptr_t)(e0 + 61440);
#pragma unroll
    for (int c = 1; c < 4; ++c) {
      const f32x4 u = *(const LAS f32x4*)(uintptr_t)(lds0 + 81920 + (c ^ w) * 15360 + ((c - 1) * 5 + q) * 1024 + lane * 16);
      if (c & 1) s1 += u; else s0 += u;
    }
    v[q] = s0 + s1;
  }
  TS(5);
  if constexpr (!LAST) {
    asm volatile("" : "+v"(res[0]), "+v"(res[1]), "+v"(res[2]), "+v"(res[3]), "+v"(res[4]));
    float sq = 0.f;
    char* o = (char*)Y + (size_t)(mt * 4 + w) * XP + nt * 2560 + lane * 8;
#pragma unroll
    for (int q = 0; q < 5; ++q) {
      f32x4 u = unpack4(res[q]) + v[q];
      wst(o + q * 512, pack4(u));
      if (q == 0 && nt == 0 && g == 0) u[1] = 0.f;
      sq += (u[0] * u[0] + u[1] * u[1]) + (u[2] * u[2] + u[3] * u[3]);
    }
    sq += __shfl_xor(sq, 16);
    sq += __shfl_xor(sq, 32);
    if (g == 0) wst(part + nt * S_ + mt * 64 + w * 16 + i, sq);
  } else {
    u16* o = Y + (size_t)row_of(mt * 64 + w * 16 + i) * D_ + nt * 80;
#pragma unroll
    for (int q = 0; q < 4; q += 2) {
      const u32x2 p0 = pack4(v[q]), p1 = pack4(v[q + 1]);
      const auto s0 = __builtin_amdgcn_permlane16_swap(p0[0], p1[0], false, false);
      const auto s1 = __builtin_amdgcn_permlane16_swap(p0[1], p1[1], false, false);
      wst(o + (q + (g & 1)) * 16 + (g >> 1) * 8, u32x4{s0[0], s1[0], s0[1], s1[1]});
    }
    wst(o + 64 + g * 4, pack4(v[4]));
  }
  TSOUT;
}

extern "C" void kernel_launch(void* const* d_in, const int* in_sizes, int n_in, void* d_out, int out_size, void* d_ws, size_t ws_size,
                              hipStream_t stream) {
  const u16* x = (const u16*)d_in[0];
  const u16* wr = (const u16*)d_in[1];
  const u16* wi = (const u16*)d_in[2];
  const u16* w1 = (const u16*)d_in[4];
  const u16* w2 = (const u16*)d_in[5];
  char* ws = (char*)d_ws;
  constexpr size_t XB = (size_t)XP * 128;
  u16* X0T = (u16*)(ws + 0 * XB);
  u16* X1T = (u16*)(ws + 1 * XB);
  u16* X2T = (u16*)(ws + 2 * XB);
  u16* X3T = (u16*)(ws + 3 * XB);
  unsigned* WT = (unsigned*)(ws + 5 * XB);
  u16* W1F = (u16*)(ws + 6 * XB);
  u16* W2F = (u16*)(ws + 7 * XB);
  u16* W2P = (u16*)(ws + 8 * XB);
  u16* H = (u16*)(ws + 9 * XB);
  u16* H2 = (u16*)(ws + 13 * XB);
  float* part0 = (float*)(ws + 12 * XB);
  float* part = part0 + 8 * S_;

  (void)hipFuncSetAttribute((const void*)k3_gemm1, hipFuncAttributeMaxDynamicSharedMemorySize, G1_NS * 8192);
  (void)hipFuncSetAttribute((const void*)k4_gemm2<false>, hipFuncAttributeMaxDynamicSharedMemorySize, G2_LDS);
  (void)hipFuncSetAttribute((const void*)k4_gemm2<true>, hipFuncAttributeMaxDynamicSharedMemorySize, G2_LDS);

  k1_prep<<<K1_X + K1_WT + K1_PF, 512, 32 * RP, stream>>>(x, wr, wi, w1, w2, X0T, part0, WT DBGPASS(0));
  k2_cols<false><<<160 + 96, 512, C_LDS, stream>>>(X0T, part0, WT, X1T, w1, W1F, w2, W2F DBGPASS(1));
  k3_gemm1<<<256, 512, G1_NS * 8192, stream>>>(X1T, W1F, H DBGPASS(2));
  k4_gemm2<false><<<256, 512, G2_LDS, stream>>>(H, W2F, X1T, X2T, part DBGPASS(3));
  k2_cols<true><<<160 + 48 + 25, 512, C_LDS, stream>>>(X2T, part, WT, X3T, w2, W2P, w2, W2P DBGPASS(4));
  k3_gemm1<<<256, 512, G1_NS * 8192, stream>>>(X3T, W1F, H2 DBGPASS(5));
  k4_gemm2<true><<<256, 512, G2_LDS, stream>>>(H2, W2P, X3T, (u16*)d_out, part DBGPASS(6));
}
```

```cpp
#include <hip/hip_runtime.h>
#include <hip/hip_bf16.h>
#include <stdint.h>
#include <utility>

typedef __bf16 bf16x8 __attribute__((ext_vector_type(8)));
typedef __bf16 bf16x4 __attribute__((ext_vector_type(4)));
typedef __bf16 bf16x2 __attribute__((ext_vector_type(2)));
typedef short s16x4 __attribute__((ext_vector_type(4)));
typedef short s16x8 __attribute__((ext_vector_type(8)));
typedef float f32x4 __attribute__((ext_vector_type(4)));
typedef float f32x2 __attribute__((ext_vector_type(2)));
typedef unsigned u32x4 __attribute__((ext_vector_type(4)));
typedef unsigned u32x2 __attribute__((ext_vector_type(2)));
typedef unsigned short u16;

#define DI __device__ __forceinline__
#define LAS __attribute__((address_space(3)))

#define DBGARG
#define TSDECL
#define TSREF
#define TSPASS
#define TS(I)
#define TSOUT
#define DBGPASS(K)
constexpr int S_ = 2048, D_ = 640, FF_ = 1536;
constexpr int XP = 161 * 128;
constexpr float EPS_ = 1e-5f;
#define PFO 80
#define PADW 1
constexpr int K1S = 20 + PADW, K2S = 48 + PADW, B1S = K1S * 1024, B2S = K2S * 1024;

namespace tab {
constexpr double kPi = 3.14159265358979323846264338327950288;
constexpr double ccos(double x) {
  double x2 = x * x, term = 1.0, sum = 1.0;
  for (int n = 1; n <= 24; ++n) { term *= -x2 / (double)((2 * n - 1) * (2 * n)); sum += term; }
  return sum;
}
constexpr uint16_t tobf(double v) {
  float f = (float)v;
  uint32_t u = __builtin_bit_cast(uint32_t, f);
  u += 0x7fffu + ((u >> 16) & 1u);
  return (uint16_t)(u >> 16);
}
struct CosD { double c[640]; };
constexpr CosD mkcosd() {
  CosD t{};
  for (int j = 0; j < 640; ++j) {
    int jj = j <= 320 ? j : 640 - j;
    t.c[j] = (jj == 160) ? 0.0 : ccos(2.0 * kPi * jj / 640.0);
  }
  return t;
}
constexpr CosD kCd = mkcosd();
constexpr double cs(int n) { return kCd.c[n % 640]; }
constexpr double sn(int n) { return kCd.c[(n + 480) % 640]; }
constexpr double kS2 = 0.05590169943749474241, kS1 = 0.03952847075210474165;
constexpr int bin_of(int cp) {
  int G = cp >> 4, t = (cp >> 3) & 1, j = cp & 7;
  return (t ? (G ? 40 - G : 20) : G) + 40 * j;
}
constexpr double aval(int r, int d1) {
  if (r >= 40 || d1 >= 40) return 0.0;
  int G = r >> 1, p = r & 1;
  if (G == 0) return (p && (d1 & 1)) ? -0.25 : 0.25;
  return p ? -0.25 * sn(16 * d1 * G) : 0.25 * cs(16 * d1 * G);
}
constexpr double cval(int G, int t, int o, int d2, int p) {
  int j = o >> 1, po = o & 1, c = bin_of(16 * G + 8 * t + j);
  if (G == 0) {
    if (p != t) return 0.0;
    if (c == 0) return (po == 0 && (d2 & 1)) ? -4.0 * kS1 : 4.0 * kS1;
    return 4.0 * kS2 * (po ? -sn(d2 * c) : cs(d2 * c));
  }
  double v = po == 0 ? (p == 0 ? cs(d2 * c) : sn(d2 * c)) : (p == 0 ? -sn(d2 * c) : cs(d2 * c));
  if (t && p) v = -v;
  return 4.0 * kS2 * v;
}
struct Fft { uint16_t c[40][64][8]; uint16_t a[6][64][8]; uint16_t ic[40][64][8]; uint16_t ia[6][64][8]; };
constexpr Fft mkfft() {
  Fft f{};
  for (int l = 0; l < 64; ++l) {
    int g = l >> 4, i = l & 15;
    for (int u = 0; u < 40; ++u)
      for (int e = 0; e < 8; ++e) f.c[u][l][e] = tobf(cval(u >> 1, u & 1, i, 4 * g + (e >> 1), e & 1));
    for (int u = 0; u < 40; ++u)
      for (int e = 0; e < 8; ++e) f.ic[u][l][e] = tobf(cval(u % 20, e >> 2, 4 * g + (e & 3), 8 * (u / 20) + (i >> 1), i & 1));
    for (int T = 0; T < 3; ++T) {
      for (int e = 0; e < 8; ++e) f.a[T][l][e] = tobf(aval(16 * T + i, 16 * (e >> 2) + 4 * g + (e & 3)));
      for (int e = 4; e < 8; ++e) f.a[3 + T][l][e] = tobf(aval(16 * T + i, 28 + 4 * g + e));
      for (int e = 0; e < 8; ++e) f.ia[2 * T][l][e] = tobf(aval(8 * g + e, 16 * T + i));
      for (int e = 0; e < 8; ++e) f.ia[2 * T + 1][l][e] = g == 3 ? tobf(aval(32 + e, 16 * T + i)) : 0;
    }
  }
  return f;
}
}
#if defined(__HIP_DEVICE_COMPILE__)
static __device__ const __attribute__((aligned(1024))) tab::Fft g_FFT = tab::mkfft();
#else
static __device__ const __attribute__((aligned(1024))) tab::Fft g_FFT = {};
#endif

template <int... I, class F> DI void sfor_impl(std::integer_sequence<int, I...>, F&& f) {
  (f(std::integral_constant<int, I>{}), ...);
}
template <int N, class F> DI void sfor(F&& f) { sfor_impl(std::make_integer_sequence<int, N>{}, f); }

DI f32x4 mfma16(bf16x8 a, bf16x8 b, f32x4 c) { return __builtin_amdgcn_mfma_f32_16x16x32_bf16(a, b, c, 0, 0, 0); }

template <int OFF> DI void glds16(const void* sbase, unsigned voff, unsigned lds_dst) {
  unsigned keep;
  asm volatile("s_mov_b32 %0, m0\n\ts_mov_b32 m0, %3\n\ts_nop 0\n\tglobal_load_lds_dwordx4 %1, %2 offset:%4\n\ts_mov_b32 m0, %0"
               : "=&s"(keep) : "v"(voff), "s"(sbase), "s"(lds_dst), "n"(OFF) : "memory");
}
#define PRE_ 3
constexpr int last_issued(int b, int NS, int T) {
  int l = (PRE_ < NS ? PRE_ : NS) - 1;
  for (int k = 0; k < b; ++k) {
    int n = l + 2;
    if (n > k - 1 + NS) n = k - 1 + NS;
    if (n > l) l = n;
  }
  return l < T - 1 ? l : T - 1;
}
template <int N> DI void wait_bar() {
  asm volatile("s_waitcnt vmcnt(%0) lgkmcnt(0)\n\ts_barrier" ::"n"(N) : "memory");
}
DI void lgkm_bar() { asm volatile("s_waitcnt lgkmcnt(0)\n\ts_barrier" ::: "memory"); }
DI bf16x8 lds128(unsigned addr) { return *(const LAS bf16x8*)(uintptr_t)addr; }
DI bf16x8 lds_tr(unsigned a0, unsigned a1) {
  s16x4 lo = __builtin_amdgcn_ds_read_tr16_b64_v4i16((LAS s16x4*)(uintptr_t)a0);
  s16x4 hi = __builtin_amdgcn_ds_read_tr16_b64_v4i16((LAS s16x4*)(uintptr_t)a1);
  s16x8 v = __builtin_shufflevector(lo, hi, 0, 1, 2, 3, 4, 5, 6, 7);
  return __builtin_bit_cast(bf16x8, v);
}
DI unsigned pack2(float a, float b) {
  f32x2 f = {a, b};
  return __builtin_bit_cast(unsigned, __builtin_convertvector(f, bf16x2));
}
DI u32x2 pack4(f32x4 v) { return u32x2{pack2(v[0], v[1]), pack2(v[2], v[3])}; }
DI bf16x8 pack8(f32x4 a, f32x4 b) {
  u32x4 u = {pack2(a[0], a[1]), pack2(a[2], a[3]), pack2(b[0], b[1]), pack2(b[2], b[3])};
  return __builtin_bit_cast(bf16x8, u);
}
DI float bflo(unsigned u) { return __builtin_bit_cast(float, u << 16); }
DI float bfhi(unsigned u) { return __builtin_bit_cast(float, u & 0xffff0000u); }
DI f32x4 unpack4(u32x2 u) { return f32x4{bflo(u[0]), bfhi(u[0]), bflo(u[1]), bfhi(u[1])}; }
DI bf16x8 neg8(bf16x8 v) {
  u32x4 u = __builtin_bit_cast(u32x4, v);
  u ^= 0x80008000u;
  return __builtin_bit_cast(bf16x8, u);
}
DI int row_of(int m) { return 32 * (m & 63) + (m >> 6); }
DI int uni(int v) { return __builtin_amdgcn_readfirstlane(v); }
template <class T> DI void wst(void* p, T v) {
  static_assert(sizeof(T) == 16 || sizeof(T) == 8 || sizeof(T) == 4, "");
  if constexpr (sizeof(T) == 16) asm volatile("global_store_dwordx4 %0, %1, off sc1\n\ts_nop 1" ::"v"(p), "v"(v) : "memory");
  else if constexpr (sizeof(T) == 8) asm volatile("global_store_dwordx2 %0, %1, off sc1" ::"v"(p), "v"(v) : "memory");
  else asm volatile("global_store_dword %0, %1, off sc1" ::"v"(p), "v"(v) : "memory");
}

template <int OFF> DI void gld16(bf16x8& dst, const void* sbase, unsigned voff) {
  asm volatile("global_load_dwordx4 %0, %1, %2 offset:%3" : "=v"(dst) : "v"(voff), "s"(sbase), "n"(OFF) : "memory");
}
template <int N> DI void qwait(bf16x8& a, bf16x8& b) { asm volatile("s_waitcnt vmcnt(%2)" : "+v"(a), "+v"(b) : "n"(N) : "memory"); }
template <int N> DI void qwait(bf16x8& a, bf16x8& b, bf16x8& c) {
  asm volatile("s_waitcnt vmcnt(%3)" : "+v"(a), "+v"(b), "+v"(c) : "n"(N) : "memory");
}
template <int N, int NT> DI void qwaitv(bf16x8 (&f)[NT]) {
  if constexpr (NT == 2) qwait<N>(f[0], f[1]); else qwait<N>(f[0], f[1], f[2]);
}

template <int NS, int T1, int PK1, int T2, int PK2>
DI void p_loader(unsigned lds0, int w, int lane, const char* p1base, unsigned p1v0, unsigned p1v1, unsigned p1step,
                 const char* p2base, unsigned p2v0, unsigned p2v1, unsigned p2step) {
  constexpr int T = T1 + T2;
  auto issue = [&](auto tc) {
    constexpr int t = decltype(tc)::value;
    const unsigned slot = lds0 + (t % NS) * 8192;
    constexpr bool ph1 = t < T1;
    constexpr int tt = ph1 ? t : t - T1;
    constexpr int PK = ph1 ? PK1 : PK2;
    const char* pb = (ph1 ? p1base : p2base) + (size_t)tt * (ph1 ? p1step : p2step);
    if constexpr (PK == 1) {
      glds16<0>(pb, ph1 ? p1v0 : p2v0, slot + w * 2048);
      glds16<1024>(pb, ph1 ? p1v0 : p2v0, slot + w * 2048);
    } else {
      glds16<0>(pb, ph1 ? p1v0 : p2v0, slot + w * 2048);
      glds16<0>(pb, ph1 ? p1v1 : p2v1, slot + w * 2048 + 1024);
    }
  };
  sfor<last_issued(0, NS, T) + 1>(issue);
  sfor<T>([&](auto tc) {
    constexpr int b = decltype(tc)::value;
    constexpr int last = last_issued(b, NS, T), nxt = last_issued(b + 1, NS, T);
    wait_bar<2 * (last - b)>();
    sfor<nxt - last>([&](auto kc) { issue(std::integral_constant<int, last + 1 + decltype(kc)::value>{}); });
  });
}
constexpr int RP = 1296;
template <int OFF, class T> DI void gldv(T& dst, const void* sbase, unsigned voff) {
  asm volatile("global_load_dwordx4 %0, %1, %2 offset:%3" : "=v"(dst) : "v"(voff), "s"(sbase), "n"(OFF) : "memory");
}
typedef short s16x12 __attribute__((ext_vector_type(12)));
DI s16x4 lds_tr4(unsigned a) { return __builtin_amdgcn_ds_read_tr16_b64_v4i16((LAS s16x4*)(uintptr_t)a); }
template <int CTRL> DI float dpp_add(float v) {
  return v + __builtin_bit_cast(float, __builtin_amdgcn_update_dpp(0, __builtin_bit_cast(int, v), CTRL, 0xf, 0xf, true));
}
DI float row16_sum(float v) {
  return dpp_add<0x121>(dpp_add<0x122>(dpp_add<0x124>(dpp_add<0x128>(v))));
}
DI void fft_step_a(f32x4 (&a)[3], const bf16x8 (&ta)[6], unsigned a0, unsigned a1, unsigned a2) {
  const s16x4 z0 = lds_tr4(a0), z1 = lds_tr4(a1), z2 = lds_tr4(a2);
  const bf16x8 lo = __builtin_bit_cast(bf16x8, __builtin_shufflevector(z0, z1, 0, 1, 2, 3, 4, 5, 6, 7));
  const bf16x8 hi = __builtin_bit_cast(bf16x8, __builtin_shufflevector(z1, z2, 0, 1, 2, 3, 4, 5, 6, 7));
#pragma unroll
  for (int T = 0; T < 3; ++T) a[T] = mfma16(ta[T], lo, f32x4{});
#pragma unroll
  for (int T = 0; T < 3; ++T) a[T] = mfma16(ta[3 + T], hi, a[T]);
}

template <int MODE>
DI void fft640_block(int blk, unsigned lds0, const u16* __restrict__ src, u16* __restrict__ dst, float* __restrict__ part TSREF) {
  const int lane = threadIdx.x & 63, w = uni(threadIdx.x >> 6), g = lane >> 4, i = lane & 15;
  const unsigned lane16 = lane * 16;
  if constexpr (MODE != 2) {
#pragma unroll
    for (int r = 0; r < 4; ++r) {
      const int rho = 4 * w + r;
      const int grow = MODE == 0 ? row_of(blk * 32 + rho) : blk * 32 + rho;
      glds16<0>((const char*)src + (size_t)grow * 1280, lane16, lds0 + rho * RP);
    }
    if (lane < 16) {
#pragma unroll
      for (int r = 0; r < 4; ++r) {
        const int rho = 4 * w + r;
        const int grow = MODE == 0 ? row_of(blk * 32 + rho) : blk * 32 + rho;
        glds16<1024>((const char*)src + (size_t)grow * 1280, lane16, lds0 + rho * RP);
      }
    }
  } else {
    const unsigned vo = (lane >> 2) * (FF_ * 2) + (lane & 3) * 16;
#pragma unroll
    for (int p = 0; p < 5; ++p)
      glds16<0>((const char*)src + blk * 64 + (size_t)(5 * w + p) * (16 * FF_ * 2), vo, lds0 + (5 * w + p) * 1088);
  }
  if constexpr (MODE == 3) {
    wait_bar<0>();
    const unsigned rb = lds0 + (4 * g + (i >> 2)) * RP + (i & 3) * 8 + w * 160;
#pragma unroll
    for (int q = 0; q < 5; ++q)
      wst((char*)dst + ((size_t)((5 * w + q) * K2S + blk) * 64 + lane) * 16, lds_tr(rb + q * 32, rb + q * 32 + 16 * RP));
    return;
  }
  const char* tb = (const char*)&g_FFT;
  constexpr int NC = MODE == 2 ? 10 : 5;
  bf16x8 ta[6], tc[NC];
  sfor<6>([&](auto uc) { constexpr int u = decltype(uc)::value; gld16<(u & 3) * 1024>(ta[u], tb + 40960 + (u >> 2) * 4096, lane16); });
  sfor<NC>([&](auto uc) {
    constexpr int u = decltype(uc)::value;
    gld16<(u & 3) * 1024>(tc[u], tb + (MODE == 2 ? (w >> 1) * 10240 : w * 5120) + (u >> 2) * 4096, lane16);
  });
  asm volatile("s_waitcnt vmcnt(%6)" : "+v"(ta[0]), "+v"(ta[1]), "+v"(ta[2]), "+v"(ta[3]), "+v"(ta[4]), "+v"(ta[5]) : "n"(NC) : "memory");
  if constexpr (MODE == 2) lgkm_bar();
  TS(1);
  if constexpr (MODE < 2) {
    f32x4 a[4][3];
#pragma unroll
    for (int r = 0; r < 4; ++r) {
      const unsigned rb = lds0 + (4 * w + r) * RP;
      fft_step_a(a[r], ta, rb + lane * 8, rb + 512 + lane * 8, rb + 1024 + (lane & 31) * 8);
    }
    const unsigned wb = lds0 + 4 * w * RP + g * 128 + i * 4;
#pragma unroll
    for (int r = 0; r < 4; ++r)
#pragma unroll
      for (int T = 0; T < 2; ++T) {
        *(LAS unsigned*)(uintptr_t)(wb + r * RP + T * 512) = pack2(a[r][T][0], a[r][T][1]);
        *(LAS unsigned*)(uintptr_t)(wb + r * RP + T * 512 + 64) = pack2(a[r][T][2], a[r][T][3]);
      }
    if (g < 2) {
#pragma unroll
      for (int r = 0; r < 4; ++r) {
        *(LAS unsigned*)(uintptr_t)(wb + r * RP + 1024) = pack2(a[r][2][0], a[r][2][1]);
        *(LAS unsigned*)(uintptr_t)(wb + r * RP + 1024 + 64) = pack2(a[r][2][2], a[r][2][3]);
      }
    }
  } else {
    const int j = w & 3, ft = w >> 2;
    const unsigned rb = lds0 + (4 * g + (i >> 2)) * 1088 + j * 256 + (i & 3) * 8 + ft * 32;
    const unsigned rt = lds0 + (32 + 4 * (g & 1) + (i >> 2)) * 1088 + j * 256 + (i & 3) * 8 + ft * 32;
    f32x4 a[4][3];
#pragma unroll
    for (int dd = 0; dd < 4; ++dd) fft_step_a(a[dd], ta, rb + dd * 64, rb + dd * 64 + 16 * 1088, rt + dd * 64);
    u32x4 u[3][2];
#pragma unroll
    for (int T = 0; T < 3; ++T)
#pragma unroll
      for (int dd = 0; dd < 4; ++dd) {
        u[T][0][dd] = pack2(a[dd][T][0], a[dd][T][1]);
        u[T][1][dd] = pack2(a[dd][T][2], a[dd][T][3]);
      }
    lgkm_bar();
    const unsigned wb = lds0 + (16 * ft + i) * RP + g * 128 + j * 16;
#pragma unroll
    for (int T = 0; T < 3; ++T)
      if (T < 2 || g < 2) {
        *(LAS u32x4*)(uintptr_t)(wb + T * 512) = u[T][0];
        *(LAS u32x4*)(uintptr_t)(wb + T * 512 + 64) = u[T][1];
      }
  }
  TS(2);
  lgkm_bar();
  asm volatile("s_waitcnt vmcnt(0)" : "+v"(tc[0]), "+v"(tc[1]), "+v"(tc[2]), "+v"(tc[3]), "+v"(tc[4])::"memory");
  TS(3);
  const unsigned db = lds0 + i * RP + g * 16;
  if constexpr (MODE == 2) {
    asm volatile("" : "+v"(tc[NC - 5]), "+v"(tc[NC - 4]), "+v"(tc[NC - 3]), "+v"(tc[NC - 2]), "+v"(tc[NC - 1]));
#pragma unroll
    for (int q = 0; q < 5; ++q) {
      const int G = 5 * (w >> 1) + q;
      const bf16x8 d = lds128(db + (w & 1) * 16 * RP + G * 64);
      f32x4 a = mfma16(tc[2 * q], d, f32x4{});
      const f32x4 b = mfma16(tc[2 * q + 1], d, f32x4{});
      if (q == 0 && w < 2 && g == 0) a[1] = 0.f;
      wst((char*)dst + ((size_t)((2 * blk + (w & 1)) * K1S + G) * 64 + lane) * 16, pack8(a, b));
    }
    return;
  }
  float sq[2] = {};
#pragma unroll
  for (int q = 0; q < 5; ++q) {
    const int u = 5 * w + q, G = u >> 1;
    const bf16x8 d0 = lds128(db + G * 64), d1 = lds128(db + 16 * RP + G * 64);
    if constexpr (MODE == 1) {
      const f32x4 a = mfma16(d0, tc[q], f32x4{}), b = mfma16(d1, tc[q], f32x4{});
      wst((char*)dst + ((size_t)(u * K2S + blk) * 64 + lane) * 16, pack8(a, b));
    } else {
      f32x4 a = mfma16(tc[q], d0, f32x4{}), b = mfma16(tc[q], d1, f32x4{});
      if constexpr (MODE == 0) {
        char* o = (char*)dst + (size_t)(2 * blk) * XP + u * 512 + lane * 8;
        wst(o, pack4(a));
        wst(o + XP, pack4(b));
      }
      if (q == 0 && w == 0 && g == 0) { a[1] = 0.f; b[1] = 0.f; }
      if constexpr (MODE == 0) {
        sq[0] += (a[0] * a[0] + a[1] * a[1]) + (a[2] * a[2] + a[3] * a[3]);
        sq[1] += (b[0] * b[0] + b[1] * b[1]) + (b[2] * b[2] + b[3] * b[3]);
      } else {
        char* o = (char*)dst + ((size_t)(2 * blk * K1S + G) * 64 + lane) * 16 + (u & 1) * 8;
        wst(o, pack4(a));
        wst(o + B1S, pack4(b));
      }
    }
  }
  if constexpr (MODE == 0) {
    float s = (g & 1) ? sq[1] : sq[0];
    s += __shfl_xor((g & 1) ? sq[0] : sq[1], 16);
    s += __shfl_xor(s, 32);
    if (g < 2) wst(part + w * S_ + blk * 32 + lane, s);
  }
}

DI void prefetch32k(const void* p, int blk) {
  u32x4 v[4];
#pragma unroll
  for (int q = 0; q < 4; ++q) gldv<0>(v[q], (const char*)p + (size_t)blk * 32768 + q * 8192, threadIdx.x * 16);
  asm volatile("s_waitcnt vmcnt(0)" : "+v"(v[0]), "+v"(v[1]), "+v"(v[2]), "+v"(v[3])::"memory");
}
#define K1_PF 120
constexpr int K1_X = 64, K1_WT = 160;
__global__ __launch_bounds__(512) void k1_prep(const u16* __restrict__ x, const u16* __restrict__ wr, const u16* __restrict__ wi,
                                              const u16* __restrict__ w1, const u16* __restrict__ w2, u16* __restrict__ X0T,
                                              float* __restrict__ part, unsigned* __restrict__ WT,
                                              const u16* __restrict__ out DBGARG) {
  TSDECL; TS(0);
  extern __shared__ __attribute__((aligned(16))) char smem[];
  const unsigned lds0 = (unsigned)(uintptr_t)(LAS char*)smem;
  const int tid = threadIdx.x;
  int b = blockIdx.x;
  if (b < K1_X) {
    fft640_block<0>(b, lds0, x, X0T, part TSPASS);
  } else if (b >= K1_X + K1_WT) {
    b -= K1_X + K1_WT;
    prefetch32k(b < 60 ? w1 : b < 120 ? w2 : out, b < 60 ? b : b < 120 ? b - 60 : b - 120);
  } else {
    b -= K1_X;
    const int kt = b % 10, ot = b / 10;
    const int kc = tid & 31, r = tid >> 5, e = r & 7;
    const int t0 = 16 * (ot >> 2) + (r >> 3) + 64 * (16 * (e >> 2) + 4 * (ot & 3) + (e & 3));
    const u16* pr = wr + t0 * 321 + kt * 32 + kc + 1;
    const u16* pi = wi + t0 * 321 + kt * 32 + kc + 1;
    unsigned v[8];
#pragma unroll
    for (int it = 0; it < 8; ++it) v[it] = (unsigned)pr[it * 642] | ((unsigned)pi[it * 642] << 16);
    const unsigned wa = lds0 + (kc * 132 + r) * 4;
#pragma unroll
    for (int it = 0; it < 8; ++it) *(LAS unsigned*)(uintptr_t)(wa + it * 64) = v[it];
    lgkm_bar();
#pragma unroll
    for (int it = 0; it < 2; ++it) {
      const int kc2 = r + 16 * it;
      const int c = (kt * 32 + kc2 + 1) % 320, rr = c % 40, j = c / 40;
      const int cp = (rr == 0 ? 0 : rr == 20 ? 8 : rr < 20 ? 16 * rr : 16 * (40 - rr) + 8) + j;
      wst(WT + cp * 2048 + ot * 128 + kc * 4, *(const LAS u32x4*)(uintptr_t)(lds0 + (kc2 * 132 + kc * 4) * 4));
    }
  }
  TSOUT;
}

struct Cx { float c, s; };
DI Cx cis(float rev) { return Cx{__builtin_amdgcn_cosf(rev), __builtin_amdgcn_sinf(rev)}; }
DI Cx cmul(Cx a, Cx b) { return Cx{a.c * b.c - a.s * b.s, a.c * b.s + a.s * b.c}; }
constexpr int C_CH = 576, C_PL = 8 * C_CH, C_OP = 32 * 272;
constexpr int C_MID = 2 * C_OP, C_HALF = C_MID + 2 * C_PL, C_LDS = 2 * C_HALF;
#define PDELAY 0
template <bool SECOND>
__global__ __launch_bounds__(512) void k2_cols(const u16* Xin, const float* __restrict__ part, const unsigned* __restrict__ WT,
                                              u16* Xout, const u16* __restrict__ wa, u16* __restrict__ Wa,
                                              const u16* __restrict__ wb, u16* __restrict__ Wb DBGARG) {
  TSDECL; TS(0);
  extern __shared__ __attribute__((aligned(16))) char smem[];
  if (blockIdx.x >= 160) {
    const unsigned l0 = (unsigned)(uintptr_t)(LAS char*)smem;
    const int b = blockIdx.x - 160;
    if (PDELAY) __builtin_amdgcn_s_sleep(PDELAY);
    if constexpr (SECOND) {
      fft640_block<3>(b, l0, wa, Wa, nullptr TSPASS);
    }
    else if (b < 48) fft640_block<2>(b, l0, wa, Wa, nullptr TSPASS);
    else fft640_block<1>(b - 48, l0, wb, Wb, nullptr TSPASS);
    TSOUT;
    return;
  }
  const int half = uni(threadIdx.x >> 8), c = 2 * blockIdx.x + half;
  const unsigned ldsb = (unsigned)(uintptr_t)(LAS char*)smem, lds0 = ldsb + half * C_HALF;
  const int tid = threadIdx.x & 255, lane = tid & 63, w = uni(tid >> 6), g = lane >> 4, i = lane & 15;
  const int T = threadIdx.x;
  f32x4 ps[8];
  u32x4 xin[2], wv[2], res[2];
  const char* xb = (const char*)Xin + blockIdx.x * 128;
  const unsigned vin = (T >> 2) * XP + (T & 3) * 32, vout = (T >> 3) * XP + (T & 7) * 16;
  gldv<0>(xin[0], xb, vin);
  gldv<16>(xin[1], xb, vin);
#pragma unroll
  for (int p = 0; p < 8; ++p) gldv<0>(ps[p], part + p * S_, T * 16);
  gldv<0>(wv[0], WT + (size_t)c * 2048, tid * 32);
  gldv<16>(wv[1], WT + (size_t)c * 2048, tid * 32);
  gldv<0>(res[0], xb, vout);
  gldv<0>(res[1], xb + 64 * XP, vout);
  __builtin_amdgcn_sched_barrier(0);
  bf16x8 f1r[2], f1i[2], f1n[2], f2r[2], f2i[2], f2n[2];
  const int k1c = 16 * w + i;
  const unsigned odd = (i & 1) ? 0x80008000u : 0u;
  {
    const Cx st = cis((float)k1c * (-1.f / 64.f));
    Cx v = cis((float)((g * k1c) & 7) * (-1.f / 8.f));
    v.c *= 0.125f; v.s *= 0.125f;
    f32x4 cr[2], ci[2];
#pragma unroll
    for (int j = 0; j < 8; ++j) {
      cr[j >> 2][j & 3] = v.c; ci[j >> 2][j & 3] = v.s;
      if (j < 7) v = cmul(v, st);
    }
    f1r[0] = pack8(cr[0], cr[1]);
    f1i[0] = pack8(ci[0], ci[1]);
    f1r[1] = __builtin_bit_cast(bf16x8, __builtin_bit_cast(u32x4, f1r[0]) ^ odd);
    f1i[1] = __builtin_bit_cast(bf16x8, __builtin_bit_cast(u32x4, f1i[0]) ^ odd);
    f1n[0] = neg8(f1i[0]);
    f1n[1] = neg8(f1i[1]);
  }
  {
    const Cx st = cis((float)i * (-1.f / 32.f));
    Cx v = cis((float)((g * i) & 7) * (-1.f / 8.f));
    v.c *= 0.17677669529663688f; v.s *= 0.17677669529663688f;
    f32x4 cr, ci;
#pragma unroll
    for (int j = 0; j < 4; ++j) {
      cr[j] = v.c; ci[j] = v.s;
      if (j < 3) v = cmul(v, st);
    }
    const u32x2 pr = pack4(cr), pi = pack4(ci);
    const u32x4 r0 = {pr[0], pr[1], pr[0] ^ odd, pr[1] ^ odd}, i0 = {pi[0], pi[1], pi[0] ^ odd, pi[1] ^ odd};
    f2r[0] = __builtin_bit_cast(bf16x8, r0);
    f2i[0] = __builtin_bit_cast(bf16x8, i0);
    f2r[1] = __builtin_bit_cast(bf16x8, r0 ^ 0x80000000u);
    f2i[1] = __builtin_bit_cast(bf16x8, i0 ^ 0x80000000u);
    f2n[0] = neg8(f2i[0]);
    f2n[1] = neg8(f2i[1]);
  }
  f32x4 twc[2], tws[2], tvc[2], tvs[2];
  {
    const Cx st = cis((float)k1c * (1.f / 2048.f)), jp = cis((float)k1c * (1.f / 128.f));
    Cx v = cis((float)(g * k1c) * (1.f / 512.f));
#pragma unroll
    for (int r = 0; r < 4; ++r) {
      const Cx u = cmul(v, jp);
      twc[0][r] = v.c; tws[0][r] = v.s; twc[1][r] = u.c; tws[1][r] = u.s;
      if (r < 3) v = cmul(v, st);
    }
#pragma unroll
    for (int mt = 0; mt < 2; ++mt) {
      const Cx s2 = cis((float)(16 * mt + i) * (1.f / 2048.f));
      Cx u = cis((float)((16 * mt + i) * (4 * w + g)) * (1.f / 512.f));
#pragma unroll
      for (int r = 0; r < 4; ++r) {
        tvc[mt][r] = u.c; tvs[mt][r] = u.s;
        if (r < 3) u = cmul(u, s2);
      }
    }
  }
  asm volatile("" : "+v"(f1r[0]), "+v"(f1r[1]), "+v"(f1i[0]), "+v"(f1i[1]), "+v"(f1n[0]), "+v"(f1n[1]), "+v"(f2r[0]), "+v"(f2r[1]),
               "+v"(f2i[0]), "+v"(f2i[1]), "+v"(f2n[0]), "+v"(f2n[1]));
  asm volatile("" : "+v"(twc[0]), "+v"(twc[1]), "+v"(tws[0]), "+v"(tws[1]), "+v"(tvc[0]), "+v"(tvc[1]), "+v"(tvs[0]), "+v"(tvs[1]));
  TS(1);
  asm volatile("s_waitcnt vmcnt(4)" : "+v"(ps[0]), "+v"(ps[1]), "+v"(ps[2]), "+v"(ps[3]), "+v"(ps[4]), "+v"(ps[5]), "+v"(ps[6]), "+v"(ps[7]),
               "+v"(xin[0]), "+v"(xin[1])::"memory");
  {
    f32x4 rs = ((ps[0] + ps[1]) + (ps[2] + ps[3])) + ((ps[4] + ps[5]) + (ps[6] + ps[7]));
#pragma unroll
    for (int e = 0; e < 4; ++e) rs[e] = rsqrtf(rs[e] * (1.f / D_) + EPS_);
    const unsigned ad = ldsb + ((T & 15) >> 1) * C_CH + (T >> 4) * 16 + (T & 1) * 8;
#pragma unroll
    for (int cc = 0; cc < 2; ++cc) {
      const unsigned x0 = xin[0][cc], x1 = xin[0][2 + cc], x2 = xin[1][cc], x3 = xin[1][2 + cc];
      const u32x2 re = {pack2(bflo(x0) * rs[0], bflo(x1) * rs[1]), pack2(bflo(x2) * rs[2], bflo(x3) * rs[3])};
      u32x2 im = {pack2(bfhi(x0) * rs[0], bfhi(x1) * rs[1]), pack2(bfhi(x2) * rs[2], bfhi(x3) * rs[3])};
      if (cc == 0 && blockIdx.x == 0) im = u32x2{0, 0};
      *(LAS u32x2*)(uintptr_t)(ad + cc * C_HALF) = re;
      *(LAS u32x2*)(uintptr_t)(ad + cc * C_HALF + C_PL) = im;
    }
  }
  asm volatile("s_waitcnt vmcnt(2) lgkmcnt(0)\n\ts_barrier" : "+v"(wv[0]), "+v"(wv[1])::"memory");
  TS(2);
  if constexpr (SECOND) {
    const int wv8 = uni(threadIdx.x >> 6), idx = blockIdx.x >> 3;
    const unsigned dump = ldsb + C_LDS + wv8 * 1024;
    if (idx * 8 + wv8 < 46) glds16<0>((const char*)&g_FFT + (46 + idx * 8 + wv8) * 1024, lane * 16, dump);
  }
  const unsigned rd = lds0 + g * C_CH + i * 16;
  {
    f32x4 ar[2] = {}, ai[2] = {};
#pragma unroll
    for (int mt = 0; mt < 2; ++mt)
#pragma unroll
      for (int ks = 0; ks < 2; ++ks) {
        const bf16x8 ur = lds128(rd + ks * 4 * C_CH + mt * 256), ui = lds128(rd + C_PL + ks * 4 * C_CH + mt * 256);
        ar[mt] = mfma16(ur, f1r[ks], ar[mt]);
        ai[mt] = mfma16(ur, f1i[ks], ai[mt]);
        ar[mt] = mfma16(ui, f1n[ks], ar[mt]);
        ai[mt] = mfma16(ui, f1r[ks], ai[mt]);
      }
    f32x4 br[2], bi[2];
#pragma unroll
    for (int mt = 0; mt < 2; ++mt) {
      br[mt] = ar[mt] * twc[mt] + ai[mt] * tws[mt];
      bi[mt] = ai[mt] * twc[mt] - ar[mt] * tws[mt];
    }
    bf16x8 pr = pack8(br[0], br[1]), pi = pack8(bi[0], bi[1]);
    f32x4 yr[2] = {}, yi[2] = {};
#pragma unroll
    for (int mt = 0; mt < 2; ++mt) {
      yr[mt] = mfma16(f2r[mt], pr, yr[mt]);
      yi[mt] = mfma16(f2i[mt], pr, yi[mt]);
      yr[mt] = mfma16(f2n[mt], pi, yr[mt]);
      yi[mt] = mfma16(f2r[mt], pi, yi[mt]);
    }
#pragma unroll
    for (int mt = 0; mt < 2; ++mt) {
      const u32x4 ww = wv[mt];
#pragma unroll
      for (int r = 0; r < 4; ++r) {
        const float wr_ = bflo(ww[r]), wi_ = bfhi(ww[r]);
        br[mt][r] = yr[mt][r] * wr_ - yi[mt][r] * wi_;
        bi[mt][r] = yr[mt][r] * wi_ + yi[mt][r] * wr_;
      }
    }
    pr = pack8(br[0], br[1]);
    pi = pack8(bi[0], bi[1]);
    f32x4 zr[2] = {}, zi[2] = {};
#pragma unroll
    for (int nt = 0; nt < 2; ++nt) {
      zr[nt] = mfma16(pr, f2r[nt], zr[nt]);
      zi[nt] = mfma16(pi, f2r[nt], zi[nt]);
      zr[nt] = mfma16(pi, f2i[nt], zr[nt]);
      zi[nt] = mfma16(pr, f2n[nt], zi[nt]);
    }
    const unsigned ad = lds0 + C_MID + (2 * w + (g >> 1)) * C_CH + i * 16 + (g & 1) * 8;
#pragma unroll
    for (int nt = 0; nt < 2; ++nt) {
      const f32x4 a = zr[nt] * tvc[nt] - zi[nt] * tvs[nt];
      const f32x4 b = zi[nt] * tvc[nt] + zr[nt] * tvs[nt];
      *(LAS u32x2*)(uintptr_t)(ad + nt * 256) = pack4(a);
      *(LAS u32x2*)(uintptr_t)(ad + C_PL + nt * 256) = pack4(b);
    }
  }
  lgkm_bar();
  TS(3);
  {
    const unsigned r2 = rd + C_MID;
    const unsigned ad = lds0 + i * 272 + w * 64 + g * 16;
#pragma unroll
    for (int nt = 0; nt < 2; ++nt) {
      f32x4 vr = {}, vi = {};
#pragma unroll
      for (int ks = 0; ks < 2; ++ks) {
        const bf16x8 qr = lds128(r2 + ks * 4 * C_CH + nt * 256), qi = lds128(r2 + C_PL + ks * 4 * C_CH + nt * 256);
        vr = mfma16(f1r[ks], qr, vr);
        vi = mfma16(f1r[ks], qi, vi);
        vr = mfma16(f1i[ks], qi, vr);
        vi = mfma16(f1n[ks], qr, vi);
      }
      *(LAS f32x4*)(uintptr_t)(ad + nt * 16 * 272) = vr;
      *(LAS f32x4*)(uintptr_t)(ad + C_OP + nt * 16 * 272) = vi;
    }
  }
  asm volatile("s_waitcnt vmcnt(0) lgkmcnt(0)\n\ts_barrier" : "+v"(res[0]), "+v"(res[1])::"memory");
  TS(4);
  const unsigned ro = ldsb + (T >> 5) * 272 + (T & 31) * 8;
#pragma unroll
  for (int hh = 0; hh < 2; ++hh) {
    f32x2 v[4];
#pragma unroll
    for (int p = 0; p < 4; ++p) v[p] = *(const LAS f32x2*)(uintptr_t)(ro + hh * 16 * 272 + (p >> 1) * C_HALF + (p & 1) * C_OP);
    if (blockIdx.x == 0) v[1] = f32x2{0.f, 0.f};
    const u32x4 r = res[hh];
    const u32x4 o = {pack2(bflo(r[0]) + v[0][0], bfhi(r[0]) + v[1][0]), pack2(bflo(r[1]) + v[2][0], bfhi(r[1]) + v[3][0]),
                     pack2(bflo(r[2]) + v[0][1], bfhi(r[2]) + v[1][1]), pack2(bflo(r[3]) + v[2][1], bfhi(r[3]) + v[3][1])};
    wst((char*)Xout + blockIdx.x * 128 + hh * 64 * XP + vout, o);
  }
  TSOUT;
}

DI bf16x8 lds_2x64(unsigned a) {
  const u32x2 lo = *(const LAS u32x2*)(uintptr_t)a, hi = *(const LAS u32x2*)(uintptr_t)(a + 512);
  return __builtin_bit_cast(bf16x8, u32x4{lo[0], lo[1], hi[0], hi[1]});
}
constexpr int G1_EX = 81920, G1_ST = G1_EX + 64 * 1024, G1_CN = G1_ST + 512, G1_IF = G1_CN + 64, IFP = 656, G1_LDS = G1_IF + 16 * IFP;
template <bool ARRIVE> DI void g1_sync(unsigned ca, int lane) {
  if (ARRIVE && lane == 0) __hip_atomic_fetch_add((LAS unsigned*)(uintptr_t)ca, 1u, __ATOMIC_RELAXED, __HIP_MEMORY_SCOPE_WORKGROUP);
  while (uni(*(volatile LAS unsigned*)(uintptr_t)ca) < 4) {}
  asm volatile("" ::: "memory");
}
template <int KM, int K0, int NS, int XH, int XI, int D, int A, int NX, class F>
DI void g1_body(f32x4 (&acc)[3][4], f32x4& dg, unsigned lds0, int lane, int w, const char* xb, const char* wb, const char* wb2,
                F&& extra TSREF) {
  constexpr int XBAR = XI + D;
  static_assert(XBAR < XH && XI < A, "");
  constexpr auto issued = [](int s) { return s >= A || s + 1 + D > NS ? NS : s + 1 + D; };
  const unsigned lane16 = lane * 16, xd = lds0 + (w * 20 + K0) * 1024;
  const int cx = w ^ (2 * KM);
  auto xissue = [&](auto sc) {
    constexpr int s = decltype(sc)::value;
    glds16<(s & 3) * 1024>(xb + K0 * 1024 + (s >> 2) * 4096, lane16, xd + (s >> 2) * 4096);
  };
  sfor<XH>(xissue);
  if (KM == 0 && threadIdx.x < 8) *(LAS unsigned*)(uintptr_t)(lds0 + G1_CN + threadIdx.x * 4) = 0;
  lgkm_bar();
  auto gsync = [&](int n) { g1_sync<true>(lds0 + G1_CN + (KM * 2 + n) * 4, lane); };
  bf16x8 wq[NS][3], xf[2][4];
  auto wissue = [&](auto sc) {
    constexpr int s = decltype(sc)::value;
    gld16<(s & 3) * 1024>(wq[s][0], wb + K0 * 1024 + (s >> 2) * 4096, lane16);
    gld16<(s & 3) * 1024>(wq[s][1], wb + B1S + K0 * 1024 + (s >> 2) * 4096, lane16);
    gld16<(s & 3) * 1024>(wq[s][2], wb2 + K0 * 1024 + (s >> 2) * 4096, lane16);
  };
  sfor<D>(wissue);
  const unsigned dcm = lane < 16 ? 0xffffu : 0xffffffffu;
  unsigned xa[4];
#pragma unroll
  for (int c = 0; c < 4; ++c) xa[c] = lds0 + ((c ^ cx) * 20 + K0) * 1024 + lane * 8;
  auto rd = [&](auto sc) {
    constexpr int s = decltype(sc)::value;
#pragma unroll
    for (int c = 0; c < 4; ++c) xf[s & 1][c] = lds_2x64(xa[c] + s * 1024);
  };
  TS(1);
  asm volatile("s_waitcnt vmcnt(%0)" ::"n"(3 * D) : "memory");
  gsync(0);
  TS(2);
  rd(std::integral_constant<int, 0>{});
  sfor<NS>([&](auto sc) {
    constexpr int s = decltype(sc)::value;
    constexpr int have = issued(s - 1), next = issued(s);
    constexpr int pend = 3 * (have - 1 - s) + (s > XI && s < XBAR ? NS - XH : 0) + (s > A ? NX : 0);
    if constexpr (s + 1 < NS && s != XBAR) rd(std::integral_constant<int, s + 1>{});
    __builtin_amdgcn_sched_barrier(0);
    qwaitv<pend, 3>(wq[s]);
    if constexpr (s == XBAR) {
      gsync(1);
      rd(std::integral_constant<int, s + 1>{});
      __builtin_amdgcn_sched_barrier(0);
    }
    TS(8 + s);
#pragma unroll
    for (int q = 0; q < 3; ++q)
#pragma unroll
      for (int c = 0; c < 4; ++c) acc[q][c] = mfma16(wq[s][q], xf[s & 1][c], acc[q][c]);
    u32x4 u = __builtin_bit_cast(u32x4, xf[s & 1][0]);
    if constexpr (K0 + s == 0) u[0] &= dcm;
    const bf16x8 v = __builtin_bit_cast(bf16x8, u);
    dg = mfma16(v, v, dg);
    __builtin_amdgcn_sched_barrier(0);
    if constexpr (s == XI) sfor<NS - XH>([&](auto kc) { xissue(std::integral_constant<int, XH + decltype(kc)::value>{}); });
    sfor<next - have>([&](auto kc) { wissue(std::integral_constant<int, have + decltype(kc)::value>{}); });
    if constexpr (s == A) extra();
    __builtin_amdgcn_sched_barrier(0);
  });
}
#define G1P0 0, 10, 5, 1, 3, 5
#define G1P1 10, 10, 5, 1, 3, 5
DI f32x4 silu4(f32x4 v) {
  f32x4 o;
#pragma unroll
  for (int r = 0; r < 4; ++r) o[r] = v[r] * __builtin_amdgcn_rcpf(1.f + __builtin_amdgcn_exp2f(v[r] * -1.4426950408889634f));
  return o;
}
template <bool INV>
__global__ __launch_bounds__(512) void k3_gemm1(const u16* __restrict__ XT, const u16* __restrict__ W1F, u16* __restrict__ H,
                                               u16* __restrict__ Y DBGARG) {
  TSDECL; TS(0);
  extern __shared__ __attribute__((aligned(16))) char smem[];
  const unsigned lds0 = (unsigned)(uintptr_t)(LAS char*)smem;
  const int tid = threadIdx.x, lane = tid & 63, wave = uni(tid >> 6), w = wave & 3, km = wave >> 2, g = lane >> 4, i = lane & 15;
  const int b = blockIdx.x, xcd = b & 7, j = b >> 3;
  const int mt = (xcd >> 1) * 8 + (j & 7), ft = (xcd & 1) * 4 + (j >> 3);
  const int e = w & 1, hf = w >> 1, cx = w ^ (2 * km);
  const unsigned lane16 = lane * 16;
  const char* xb = (const char*)XT + (size_t)(mt * 4 + w) * XP;
  const char* wb = (const char*)W1F + (size_t)(ft * 12 + hf * 6 + 2 * e) * B1S;
  f32x4 acc[3][4] = {};
  f32x4 dg = {};
  bf16x8 tc[5], ta[4];
  auto tabs = [&]() {
    if constexpr (INV) {
      const char* tb = (const char*)&g_FFT + 46 * 1024;
      sfor<5>([&](auto uc) { constexpr int u = decltype(uc)::value; gld16<(u & 3) * 1024>(tc[u], tb + ((ft & 1) * 20 + w * 5 + (u & 4)) * 1024, lane16); });
      gld16<0>(ta[0], tb + (40 + 2 * hf) * 1024, lane16);
      gld16<1024>(ta[1], tb + (40 + 2 * hf) * 1024, lane16);
      gld16<0>(ta[2], tb + 44 * 1024, lane16);
      gld16<1024>(ta[3], tb + 44 * 1024, lane16);
    }
  };
  if (km == 0) {
    g1_body<0, G1P0, INV ? 9 : 0>(acc, dg, lds0, lane, w, xb, wb, wb + (4 - e) * B1S, tabs TSPASS);
    if constexpr (INV)
      asm volatile("s_waitcnt vmcnt(0)" : "+v"(tc[0]), "+v"(tc[1]), "+v"(tc[2]), "+v"(tc[3]), "+v"(tc[4]), "+v"(ta[0]), "+v"(ta[1]), "+v"(ta[2]),
                   "+v"(ta[3])::"memory");
  } else g1_body<1, G1P1, 0>(acc, dg, lds0, lane, w, xb, wb, wb + (4 - e) * B1S, [] {} TSPASS);
  TS(5);
  {
    const unsigned ex = lds0 + G1_EX + wave * 8192 + lane16;
#pragma unroll
    for (int c = 2; c < 4; ++c)
#pragma unroll
      for (int q = 0; q < 2; ++q) *(LAS f32x4*)(uintptr_t)(ex + ((c - 2) * 2 + q) * 1024) = acc[q][c];
#pragma unroll
    for (int c = 1; c < 4; ++c) *(LAS f32x4*)(uintptr_t)(ex + (4 + c) * 1024) = acc[2][c];
    const int q4 = i >> 2, p4 = i & 3;
    if (g == q4) {
      const float d0 = dg[0], d1 = dg[1], d2 = dg[2], d3 = dg[3];
      *(LAS float*)(uintptr_t)(lds0 + G1_ST + km * 256 + (cx * 16 + i) * 4) = p4 == 0 ? d0 : p4 == 1 ? d1 : p4 == 2 ? d2 : d3;
    }
  }
  if constexpr (INV) {
    if (km == 0) {
      g1_sync<false>(lds0 + G1_CN + 12, lane);
      const unsigned xs = lds0 + ((ft >> 1) * 20 + w * 5) * 1024 + lane * 8, mw = lds0 + G1_IF + i * IFP + g * 160 + w * 20;
#pragma unroll
      for (int q = 0; q < 5; ++q) {
        const f32x4 d = mfma16(tc[q], lds_2x64(xs + q * 1024), f32x4{});
        *(LAS unsigned*)(uintptr_t)(mw + q * 4) = pack2(d[0], d[1]);
        *(LAS unsigned*)(uintptr_t)(mw + q * 4 + 80) = pack2(d[2], d[3]);
      }
      asm volatile("s_waitcnt lgkmcnt(0)" ::: "memory");
      g1_sync<true>(lds0 + G1_CN + 16, lane);
      const unsigned mr = lds0 + G1_IF + i * IFP + e * 320 + g * 16;
      bf16x8 b0[4], b1[4];
#pragma unroll
      for (int dd = 0; dd < 4; ++dd) { b0[dd] = lds128(mr + dd * 80); b1[dd] = lds128(mr + dd * 80 + 16); }
      char* yo = (char*)Y + (size_t)(mt * 4 + (ft >> 1)) * XP + (16 * g + 2 * (ft & 1) + e) * 128 + i * 8;
      auto unit = [&](bf16x8 t0, bf16x8 t1, int T, bool st) {
        f32x4 o[4];
#pragma unroll
        for (int dd = 0; dd < 4; ++dd) o[dd] = mfma16(t1, b1[dd], mfma16(t0, b0[dd], f32x4{}));
        if (st) {
#pragma unroll
          for (int r = 0; r < 4; ++r) wst(yo + T * 8192 + r * 512, pack4(f32x4{o[0][r], o[1][r], o[2][r], o[3][r]}));
        }
      };
      unit(ta[0], ta[1], hf, true);
      if (hf == 0) unit(ta[2], ta[3], 2, g < 2);
    }
  }
  lgkm_bar();
  TS(6);
  const unsigned ep = lds0 + G1_EX + (wave ^ 4) * 8192 + lane16, en = lds0 + G1_EX + (wave ^ 1) * 8192 + lane16,
                 eo = lds0 + G1_EX + (wave ^ 5) * 8192 + lane16;
  float rstd[2];
#pragma unroll
  for (int c = 0; c < 2; ++c) {
    const unsigned sa = lds0 + G1_ST + ((c ^ cx) * 16 + i) * 4;
    rstd[c] = rsqrtf((*(const LAS float*)(uintptr_t)sa + *(const LAS float*)(uintptr_t)(sa + 256)) * (1.f / D_) + EPS_);
  }
  char* hb = (char*)H + (size_t)(mt * 4 * K2S + ft * 6 + hf * 3) * 1024 + lane16;
#pragma unroll
  for (int c = 0; c < 2; ++c) {
    const u32x2 h0 = pack4(silu4((acc[0][c] + *(const LAS f32x4*)(uintptr_t)(ep + (c * 2) * 1024)) * rstd[c]));
    const u32x2 h1 = pack4(silu4((acc[1][c] + *(const LAS f32x4*)(uintptr_t)(ep + (c * 2 + 1) * 1024)) * rstd[c]));
    wst(hb + ((c ^ cx) * K2S + e) * 1024, u32x4{h0[0], h0[1], h1[0], h1[1]});
  }
  {
    const u32x2 h0 = pack4(silu4((acc[2][0] + *(const LAS f32x4*)(uintptr_t)(ep + 6 * 1024)) * rstd[0]));
    const u32x2 h1 = pack4(silu4((*(const LAS f32x4*)(uintptr_t)(en + 5 * 1024) + *(const LAS f32x4*)(uintptr_t)(eo + 7 * 1024)) * rstd[0]));
    wst(hb + (cx * K2S + 2) * 1024, e ? u32x4{h1[0], h1[1], h0[0], h0[1]} : u32x4{h0[0], h0[1], h1[0], h1[1]});
  }
  TSOUT;
}

template <int OFF> DI void gld8(u32x2& dst, const void* sbase, unsigned voff) {
  asm volatile("global_load_dwordx2 %0, %1, %2 offset:%3" : "=v"(dst) : "v"(voff), "s"(sbase), "n"(OFF) : "memory");
}
#define G2D 4
#define G2E 5
constexpr int G2_LDS = 140 * 1024;
template <int N1, int N2>
DI void g2_stream(f32x4 (&acc)[5][4], int lane, int cx, const char* a1, const char* b1, const char* a2, const char* b2 TSREF) {
  constexpr int N = N1 + N2, D = G2D;
  bf16x8 af[D][5], bf[D][4];
  u32x2 bl[D][8];
  const unsigned l16 = lane * 16, l8 = lane * 8;
  auto issue = [&](auto sc) {
    constexpr int s = decltype(sc)::value;
    if constexpr (s < N1) {
#pragma unroll
      for (int c = 0; c < 4; ++c) {
        gld8<0>(bl[s % D][2 * c], b1 + (size_t)(c ^ cx) * XP + s * 1024, l8);
        gld8<512>(bl[s % D][2 * c + 1], b1 + (size_t)(c ^ cx) * XP + s * 1024, l8);
      }
#pragma unroll
      for (int q = 0; q < 5; ++q) gld16<0>(af[s % D][q], a1 + q * 20480 + s * 1024, l16);
    } else {
#pragma unroll
      for (int c = 0; c < 4; ++c) gld16<0>(bf[s % D][c], b2 + (size_t)(c ^ cx) * B2S + (s - N1) * 1024, l16);
#pragma unroll
      for (int q = 0; q < 5; ++q) gld16<0>(af[s % D][q], a2 + q * B2S + (s - N1) * 1024, l16);
    }
  };
  sfor<(D < N ? D : N)>(issue);
  sfor<N>([&](auto sc) {
    constexpr int s = decltype(sc)::value, last = (s + D - 1 < N - 1 ? s + D - 1 : N - 1);
    constexpr int n1 = (last < N1 ? last : N1 - 1) - s;
    constexpr int pend = (n1 > 0 ? n1 : 0) * 13 + ((last - s) - (n1 > 0 ? n1 : 0)) * 9;
    bf16x8(&a)[5] = af[s % D];
    if constexpr (s < N1) {
      u32x2(&l)[8] = bl[s % D];
      asm volatile("s_waitcnt vmcnt(%13)" : "+v"(a[0]), "+v"(a[1]), "+v"(a[2]), "+v"(a[3]), "+v"(a[4]), "+v"(l[0]), "+v"(l[1]), "+v"(l[2]),
                   "+v"(l[3]), "+v"(l[4]), "+v"(l[5]), "+v"(l[6]), "+v"(l[7]) : "n"(pend) : "memory");
#pragma unroll
      for (int c = 0; c < 4; ++c)
        bf[s % D][c] = __builtin_bit_cast(bf16x8, u32x4{l[2 * c][0], l[2 * c][1], l[2 * c + 1][0], l[2 * c + 1][1]});
    } else {
      bf16x8(&b)[4] = bf[s % D];
      asm volatile("s_waitcnt vmcnt(%9)" : "+v"(a[0]), "+v"(a[1]), "+v"(a[2]), "+v"(a[3]), "+v"(a[4]), "+v"(b[0]), "+v"(b[1]), "+v"(b[2]),
                   "+v"(b[3]) : "n"(pend) : "memory");
    }
    TS(8 + s);
#pragma unroll
    for (int q = 0; q < 5; ++q)
#pragma unroll
      for (int c = 0; c < 4; ++c) acc[q][c] = mfma16(a[q], bf[s % D][c], acc[q][c]);
    __builtin_amdgcn_sched_barrier(0);
    if constexpr (s + D < N) issue(std::integral_constant<int, s + D>{});
    __builtin_amdgcn_sched_barrier(0);
  });
}
template <bool LAST>
__global__ __launch_bounds__(512) void k4_gemm2(const u16* __restrict__ H, const u16* __restrict__ W2, const u16* __restrict__ X,
                                               u16* __restrict__ Y, float* __restrict__ part DBGARG) {
  TSDECL; TS(0);
  extern __shared__ __attribute__((aligned(16))) char smem[];
  const unsigned lds0 = (unsigned)(uintptr_t)(LAS char*)smem;
  const int tid = threadIdx.x, lane = tid & 63, wave = uni(tid >> 6), w = wave & 3, g = lane >> 4, i = lane & 15;
  const int b = blockIdx.x, xcd = b & 7, j = b >> 3;
  const int mt = (xcd >> 1) * 8 + (j & 7), nt = (xcd & 1) * 4 + (j >> 3);
  constexpr int E2 = G2E, L2 = 12 - E2, E1 = 0, L1 = 0;
  const char* a1 = nullptr;
  const char* b1 = (const char*)X + (size_t)mt * 4 * XP;
  const char* a2 = (const char*)W2 + (size_t)nt * 5 * B2S;
  const char* b2 = (const char*)H + (size_t)mt * 4 * B2S;
  f32x4 acc[5][4] = {};
  if (wave >= 4) {
    g2_stream<E1, E2>(acc, lane, 0, a1 + (4 * L1 + w * E1) * 1024, b1 + (4 * L1 + w * E1) * 1024, a2 + (4 * L2 + w * E2) * 1024,
                      b2 + (4 * L2 + w * E2) * 1024 TSPASS);
    const unsigned ex = lds0 + w * 20480 + lane * 16;
#pragma unroll
    for (int c = 0; c < 4; ++c)
#pragma unroll
      for (int q = 0; q < 5; ++q) *(LAS f32x4*)(uintptr_t)(ex + (c * 5 + q) * 1024) = acc[q][c];
    TS(4);
    lgkm_bar();
    TS(5);
    TSOUT;
    return;
  }
  u32x2 res[5];
#pragma unroll
  for (int q = 0; q < 5; ++q) gld8<0>(res[q], b1 + (size_t)w * XP + (nt * 5 + q) * 512, lane * 8);
  g2_stream<L1, L2>(acc, lane, w, a1 + w * L1 * 1024, b1 + w * L1 * 1024, a2 + w * L2 * 1024, b2 + w * L2 * 1024 TSPASS);
  TS(4);
  {
    const unsigned ex = lds0 + 81920 + w * 15360 + lane * 16;
#pragma unroll
    for (int c = 1; c < 4; ++c)
#pragma unroll
      for (int q = 0; q < 5; ++q) *(LAS f32x4*)(uintptr_t)(ex + ((c - 1) * 5 + q) * 1024) = acc[q][c];
  }
  f32x4 v[5];
  lgkm_bar();
#pragma unroll
  for (int q = 0; q < 5; ++q) {
    const unsigned e0 = lds0 + (w * 5 + q) * 1024 + lane * 16;
    f32x4 s0 = acc[q][0] + *(const LAS f32x4*)(uintptr_t)(e0), s1 = *(const LAS f32x4*)(uintptr_t)(e0 + 20480);
    s0 += *(const LAS f32x4*)(uintptr_t)(e0 + 40960);
    s1 += *(const LAS f32x4*)(uintptr_t)(e0 + 61440);
#pragma unroll
    for (int c = 1; c < 4; ++c) {
      const f32x4 u = *(const LAS f32x4*)(uintptr_t)(lds0 + 81920 + (c ^ w) * 15360 + ((c - 1) * 5 + q) * 1024 + lane * 16);
      if (c & 1) s1 += u; else s0 += u;
    }
    v[q] = s0 + s1;
  }
  TS(5);
  asm volatile("" : "+v"(res[0]), "+v"(res[1]), "+v"(res[2]), "+v"(res[3]), "+v"(res[4]));
#pragma unroll
  for (int q = 0; q < 5; ++q) v[q] += unpack4(res[q]);
  if constexpr (!LAST) {
    float sq = 0.f;
    char* o = (char*)Y + (size_t)(mt * 4 + w) * XP + nt * 2560 + lane * 8;
#pragma unroll
    for (int q = 0; q < 5; ++q) {
      f32x4 u = v[q];
      wst(o + q * 512, pack4(u));
      if (q == 0 && nt == 0 && g == 0) u[1] = 0.f;
      sq += (u[0] * u[0] + u[1] * u[1]) + (u[2] * u[2] + u[3] * u[3]);
    }
    sq += __shfl_xor(sq, 16);
    sq += __shfl_xor(sq, 32);
    if (g == 0) wst(part + nt * S_ + mt * 64 + w * 16 + i, sq);
  } else {
    u16* o = Y + (size_t)row_of(mt * 64 + w * 16 + i) * D_ + nt * 80;
#pragma unroll
    for (int q = 0; q < 4; q += 2) {
      const u32x2 p0 = pack4(v[q]), p1 = pack4(v[q + 1]);
      const auto s0 = __builtin_amdgcn_permlane16_swap(p0[0], p1[0], false, false);
      const auto s1 = __builtin_amdgcn_permlane16_swap(p0[1], p1[1], false, false);
      wst(o + (q + (g & 1)) * 16 + (g >> 1) * 8, u32x4{s0[0], s1[0], s0[1], s1[1]});
    }
    wst(o + 64 + g * 4, pack4(v[4]));
  }
  TSOUT;
}

extern "C" void kernel_launch(void* const* d_in, const int* in_sizes, int n_in, void* d_out, int out_size, void* d_ws, size_t ws_size,
                              hipStream_t stream) {
  const u16* x = (const u16*)d_in[0];
  const u16* wr = (const u16*)d_in[1];
  const u16* wi = (const u16*)d_in[2];
  const u16* w1 = (const u16*)d_in[4];
  const u16* w2 = (const u16*)d_in[5];
  char* ws = (char*)d_ws;
  constexpr size_t XB = (size_t)XP * 128;
  u16* X0T = (u16*)(ws + 0 * XB);
  u16* X1T = (u16*)(ws + 1 * XB);
  u16* X2T = (u16*)(ws + 2 * XB);
  u16* X3T = (u16*)(ws + 3 * XB);
  u16* Y3 = (u16*)(ws + 4 * XB);
  unsigned* WT = (unsigned*)(ws + 5 * XB);
  u16* W1F = (u16*)(ws + 6 * XB);
  u16* W2F = (u16*)(ws + 7 * XB);
  u16* W2P = (u16*)(ws + 8 * XB);
  u16* H = (u16*)(ws + 9 * XB);
  u16* H2 = (u16*)(ws + 13 * XB);
  float* part0 = (float*)(ws + 12 * XB);
  float* part = part0 + 8 * S_;

  (void)hipFuncSetAttribute((const void*)k3_gemm1<false>, hipFuncAttributeMaxDynamicSharedMemorySize, G1_LDS);
  (void)hipFuncSetAttribute((const void*)k3_gemm1<true>, hipFuncAttributeMaxDynamicSharedMemorySize, G1_LDS);
  (void)hipFuncSetAttribute((const void*)k4_gemm2<false>, hipFuncAttributeMaxDynamicSharedMemorySize, G2_LDS);
  (void)hipFuncSetAttribute((const void*)k4_gemm2<true>, hipFuncAttributeMaxDynamicSharedMemorySize, G2_LDS);

  k1_prep<<<K1_X + K1_WT + K1_PF + PFO, 512, 32 * RP, stream>>>(x, wr, wi, w1, w2, X0T, part0, WT, (const u16*)d_out DBGPASS(0));
  k2_cols<false><<<160 + 96, 512, C_LDS, stream>>>(X0T, part0, WT, X1T, w1, W1F, w2, W2F DBGPASS(1));
  k3_gemm1<false><<<256, 512, G1_LDS, stream>>>(X1T, W1F, H, Y3 DBGPASS(2));
  k4_gemm2<false><<<256, 512, G2_LDS, stream>>>(H, W2F, X1T, X2T, part DBGPASS(3));
  k2_cols<true><<<160 + 48, 512, C_LDS + 8192, stream>>>(X2T, part, WT, X3T, w2, W2P, w2, W2P DBGPASS(4));
  k3_gemm1<true><<<256, 512, G1_LDS, stream>>>(X3T, W1F, H2, Y3 DBGPASS(5));
  k4_gemm2<true><<<256, 512, G2_LDS, stream>>>(H2, W2P, Y3, (u16*)d_out, part DBGPASS(6));
}
```

```cpp
#include <hip/hip_runtime.h>
#include <hip/hip_bf16.h>
#include <stdint.h>
#include <utility>

typedef __bf16 bf16x8 __attribute__((ext_vector_type(8)));
typedef __bf16 bf16x4 __attribute__((ext_vector_type(4)));
typedef __bf16 bf16x2 __attribute__((ext_vector_type(2)));
typedef short s16x4 __attribute__((ext_vector_type(4)));
typedef short s16x8 __attribute__((ext_vector_type(8)));
typedef float f32x4 __attribute__((ext_vector_type(4)));
typedef float f32x2 __attribute__((ext_vector_type(2)));
typedef unsigned u32x4 __attribute__((ext_vector_type(4)));
typedef unsigned u32x2 __attribute__((ext_vector_type(2)));
typedef unsigned short u16;

#define DI __device__ __forceinline__
#define LAS __attribute__((address_space(3)))

#define DBGARG
#define DBGFWD
#define TSDECL
#define TSREF
#define TSPASS
#define TS(I)
#define TSOUT
#define DBGPASS(K)
constexpr int S_ = 2048, D_ = 640, FF_ = 1536;
constexpr int XP = 161 * 128;
constexpr float EPS_ = 1e-5f;
#define PFO 80
#define PADW 1
constexpr int K1S = 20 + PADW, K2S = 48 + PADW, B1S = K1S * 1024, B2S = K2S * 1024;

namespace tab {
constexpr double kPi = 3.14159265358979323846264338327950288;
constexpr double ccos(double x) {
  double x2 = x * x, term = 1.0, sum = 1.0;
  for (int n = 1; n <= 24; ++n) { term *= -x2 / (double)((2 * n - 1) * (2 * n)); sum += term; }
  return sum;
}
constexpr uint16_t tobf(double v) {
  float f = (float)v;
  uint32_t u = __builtin_bit_cast(uint32_t, f);
  u += 0x7fffu + ((u >> 16) & 1u);
  return (uint16_t)(u >> 16);
}
struct CosD { double c[640]; };
constexpr CosD mkcosd() {
  CosD t{};
  for (int j = 0; j < 640; ++j) {
    int jj = j <= 320 ? j : 640 - j;
    t.c[j] = (jj == 160) ? 0.0 : ccos(2.0 * kPi * jj / 640.0);
  }
  return t;
}
constexpr CosD kCd = mkcosd();
constexpr double cs(int n) { return kCd.c[n % 640]; }
constexpr double sn(int n) { return kCd.c[(n + 480) % 640]; }
constexpr double kS2 = 0.05590169943749474241, kS1 = 0.03952847075210474165;
constexpr int bin_of(int cp) {
  int G = cp >> 4, t = (cp >> 3) & 1, j = cp & 7;
  return (t ? (G ? 40 - G : 20) : G) + 40 * j;
}
constexpr double aval(int r, int d1) {
  if (r >= 40 || d1 >= 40) return 0.0;
  int G = r >> 1, p = r & 1;
  if (G == 0) return (p && (d1 & 1)) ? -0.25 : 0.25;
  return p ? -0.25 * sn(16 * d1 * G) : 0.25 * cs(16 * d1 * G);
}
constexpr double cval(int G, int t, int o, int d2, int p) {
  int j = o >> 1, po = o & 1, c = bin_of(16 * G + 8 * t + j);
  if (G == 0) {
    if (p != t) return 0.0;
    if (c == 0) return (po == 0 && (d2 & 1)) ? -4.0 * kS1 : 4.0 * kS1;
    return 4.0 * kS2 * (po ? -sn(d2 * c) : cs(d2 * c));
  }
  double v = po == 0 ? (p == 0 ? cs(d2 * c) : sn(d2 * c)) : (p == 0 ? -sn(d2 * c) : cs(d2 * c));
  if (t && p) v = -v;
  return 4.0 * kS2 * v;
}
struct Fft { uint16_t c[40][64][8]; uint16_t a[6][64][8]; uint16_t ic[40][64][8]; uint16_t ia[6][64][8]; };
constexpr Fft mkfft() {
  Fft f{};
  for (int l = 0; l < 64; ++l) {
    int g = l >> 4, i = l & 15;
    for (int u = 0; u < 40; ++u)
      for (int e = 0; e < 8; ++e) f.c[u][l][e] = tobf(cval(u >> 1, u & 1, i, 4 * g + (e >> 1), e & 1));
    for (int u = 0; u < 40; ++u)
      for (int e = 0; e < 8; ++e) f.ic[u][l][e] = tobf(cval(u % 20, e >> 2, 4 * g + (e & 3), 8 * (u / 20) + (i >> 1), i & 1));
    for (int T = 0; T < 3; ++T) {
      for (int e = 0; e < 8; ++e) f.a[T][l][e] = tobf(aval(16 * T + i, 16 * (e >> 2) + 4 * g + (e & 3)));
      for (int e = 4; e < 8; ++e) f.a[3 + T][l][e] = tobf(aval(16 * T + i, 28 + 4 * g + e));
      for (int e = 0; e < 8; ++e) f.ia[2 * T][l][e] = tobf(aval(8 * g + e, 16 * T + i));
      for (int e = 0; e < 8; ++e) f.ia[2 * T + 1][l][e] = g == 3 ? tobf(aval(32 + e, 16 * T + i)) : 0;
    }
  }
  return f;
}
}
#if defined(__HIP_DEVICE_COMPILE__)
static __device__ const __attribute__((aligned(1024))) tab::Fft g_FFT = tab::mkfft();
#else
static __device__ const __attribute__((aligned(1024))) tab::Fft g_FFT = {};
#endif

template <int... I, class F> DI void sfor_impl(std::integer_sequence<int, I...>, F&& f) {
  (f(std::integral_constant<int, I>{}), ...);
}
template <int N, class F> DI void sfor(F&& f) { sfor_impl(std::make_integer_sequence<int, N>{}, f); }

DI f32x4 mfma16(bf16x8 a, bf16x8 b, f32x4 c) { return __builtin_amdgcn_mfma_f32_16x16x32_bf16(a, b, c, 0, 0, 0); }

template <int OFF> DI void glds16(const void* sbase, unsigned voff, unsigned lds_dst) {
  unsigned keep;
  asm volatile("s_mov_b32 %0, m0\n\ts_mov_b32 m0, %3\n\ts_nop 0\n\tglobal_load_lds_dwordx4 %1, %2 offset:%4\n\ts_mov_b32 m0, %0"
               : "=&s"(keep) : "v"(voff), "s"(sbase), "s"(lds_dst), "n"(OFF) : "memory");
}
#define PRE_ 3
constexpr int last_issued(int b, int NS, int T) {
  int l = (PRE_ < NS ? PRE_ : NS) - 1;
  for (int k = 0; k < b; ++k) {
    int n = l + 2;
    if (n > k - 1 + NS) n = k - 1 + NS;
    if (n > l) l = n;
  }
  return l < T - 1 ? l : T - 1;
}
template <int N> DI void wait_bar() {
  asm volatile("s_waitcnt vmcnt(%0) lgkmcnt(0)\n\ts_barrier" ::"n"(N) : "memory");
}
DI void lgkm_bar() { asm volatile("s_waitcnt lgkmcnt(0)\n\ts_barrier" ::: "memory"); }
DI bf16x8 lds128(unsigned addr) { return *(const LAS bf16x8*)(uintptr_t)addr; }
DI bf16x8 lds_tr(unsigned a0, unsigned a1) {
  s16x4 lo = __builtin_amdgcn_ds_read_tr16_b64_v4i16((LAS s16x4*)(uintptr_t)a0);
  s16x4 hi = __builtin_amdgcn_ds_read_tr16_b64_v4i16((LAS s16x4*)(uintptr_t)a1);
  s16x8 v = __builtin_shufflevector(lo, hi, 0, 1, 2, 3, 4, 5, 6, 7);
  return __builtin_bit_cast(bf16x8, v);
}
DI unsigned pack2(float a, float b) {
  f32x2 f = {a, b};
  return __builtin_bit_cast(unsigned, __builtin_convertvector(f, bf16x2));
}
DI u32x2 pack4(f32x4 v) { return u32x2{pack2(v[0], v[1]), pack2(v[2], v[3])}; }
DI bf16x8 pack8(f32x4 a, f32x4 b) {
  u32x4 u = {pack2(a[0], a[1]), pack2(a[2], a[3]), pack2(b[0], b[1]), pack2(b[2], b[3])};
  return __builtin_bit_cast(bf16x8, u);
}
DI float bflo(unsigned u) { return __builtin_bit_cast(float, u << 16); }
DI float bfhi(unsigned u) { return __builtin_bit_cast(float, u & 0xffff0000u); }
DI f32x4 unpack4(u32x2 u) { return f32x4{bflo(u[0]), bfhi(u[0]), bflo(u[1]), bfhi(u[1])}; }
DI bf16x8 neg8(bf16x8 v) {
  u32x4 u = __builtin_bit_cast(u32x4, v);
  u ^= 0x80008000u;
  return __builtin_bit_cast(bf16x8, u);
}
DI int row_of(int m) { return 32 * (m & 63) + (m >> 6); }
DI int uni(int v) { return __builtin_amdgcn_readfirstlane(v); }
template <class T> DI void wst(void* p, T v) {
  static_assert(sizeof(T) == 16 || sizeof(T) == 8 || sizeof(T) == 4, "");
  if constexpr (sizeof(T) == 16) asm volatile("global_store_dwordx4 %0, %1, off sc1\n\ts_nop 1" ::"v"(p), "v"(v) : "memory");
  else if constexpr (sizeof(T) == 8) asm volatile("global_store_dwordx2 %0, %1, off sc1" ::"v"(p), "v"(v) : "memory");
  else asm volatile("global_store_dword %0, %1, off sc1" ::"v"(p), "v"(v) : "memory");
}

template <int OFF> DI void gld16(bf16x8& dst, const void* sbase, unsigned voff) {
  asm volatile("global_load_dwordx4 %0, %1, %2 offset:%3" : "=v"(dst) : "v"(voff), "s"(sbase), "n"(OFF) : "memory");
}
template <int N> DI void qwait(bf16x8& a, bf16x8& b) { asm volatile("s_waitcnt vmcnt(%2)" : "+v"(a), "+v"(b) : "n"(N) : "memory"); }
template <int N> DI void qwait(bf16x8& a, bf16x8& b, bf16x8& c) {
  asm volatile("s_waitcnt vmcnt(%3)" : "+v"(a), "+v"(b), "+v"(c) : "n"(N) : "memory");
}
template <int N, int NT> DI void qwaitv(bf16x8 (&f)[NT]) {
  if constexpr (NT == 2) qwait<N>(f[0], f[1]); else qwait<N>(f[0], f[1], f[2]);
}

template <int NS, int T1, int PK1, int T2, int PK2>
DI void p_loader(unsigned lds0, int w, int lane, const char* p1base, unsigned p1v0, unsigned p1v1, unsigned p1step,
                 const char* p2base, unsigned p2v0, unsigned p2v1, unsigned p2step) {
  constexpr int T = T1 + T2;
  auto issue = [&](auto tc) {
    constexpr int t = decltype(tc)::value;
    const unsigned slot = lds0 + (t % NS) * 8192;
    constexpr bool ph1 = t < T1;
    constexpr int tt = ph1 ? t : t - T1;
    constexpr int PK = ph1 ? PK1 : PK2;
    const char* pb = (ph1 ? p1base : p2base) + (size_t)tt * (ph1 ? p1step : p2step);
    if constexpr (PK == 1) {
      glds16<0>(pb, ph1 ? p1v0 : p2v0, slot + w * 2048);
      glds16<1024>(pb, ph1 ? p1v0 : p2v0, slot + w * 2048);
    } else {
      glds16<0>(pb, ph1 ? p1v0 : p2v0, slot + w * 2048);
      glds16<0>(pb, ph1 ? p1v1 : p2v1, slot + w * 2048 + 1024);
    }
  };
  sfor<last_issued(0, NS, T) + 1>(issue);
  sfor<T>([&](auto tc) {
    constexpr int b = decltype(tc)::value;
    constexpr int last = last_issued(b, NS, T), nxt = last_issued(b + 1, NS, T);
    wait_bar<2 * (last - b)>();
    sfor<nxt - last>([&](auto kc) { issue(std::integral_constant<int, last + 1 + decltype(kc)::value>{}); });
  });
}
constexpr int RP = 1296;
template <int OFF, class T> DI void gldv(T& dst, const void* sbase, unsigned voff) {
  asm volatile("global_load_dwordx4 %0, %1, %2 offset:%3" : "=v"(dst) : "v"(voff), "s"(sbase), "n"(OFF) : "memory");
}
typedef short s16x12 __attribute__((ext_vector_type(12)));
DI s16x4 lds_tr4(unsigned a) { return __builtin_amdgcn_ds_read_tr16_b64_v4i16((LAS s16x4*)(uintptr_t)a); }
template <int CTRL> DI float dpp_add(float v) {
  return v + __builtin_bit_cast(float, __builtin_amdgcn_update_dpp(0, __builtin_bit_cast(int, v), CTRL, 0xf, 0xf, true));
}
DI float row16_sum(float v) {
  return dpp_add<0x121>(dpp_add<0x122>(dpp_add<0x124>(dpp_add<0x128>(v))));
}
DI void fft_step_a(f32x4 (&a)[3], const bf16x8 (&ta)[6], unsigned a0, unsigned a1, unsigned a2) {
  const s16x4 z0 = lds_tr4(a0), z1 = lds_tr4(a1), z2 = lds_tr4(a2);
  const bf16x8 lo = __builtin_bit_cast(bf16x8, __builtin_shufflevector(z0, z1, 0, 1, 2, 3, 4, 5, 6, 7));
  const bf16x8 hi = __builtin_bit_cast(bf16x8, __builtin_shufflevector(z1, z2, 0, 1, 2, 3, 4, 5, 6, 7));
#pragma unroll
  for (int T = 0; T < 3; ++T) a[T] = mfma16(ta[T], lo, f32x4{});
#pragma unroll
  for (int T = 0; T < 3; ++T) a[T] = mfma16(ta[3 + T], hi, a[T]);
}

template <int MODE>
DI void fft640_block(int blk, unsigned lds0, const u16* __restrict__ src, u16* __restrict__ dst, float* __restrict__ part TSREF) {
  const int lane = threadIdx.x & 63, w = uni(threadIdx.x >> 6), g = lane >> 4, i = lane & 15;
  const unsigned lane16 = lane * 16;
  if constexpr (MODE != 2) {
#pragma unroll
    for (int r = 0; r < 4; ++r) {
      const int rho = 4 * w + r;
      const int grow = MODE == 0 ? row_of(blk * 32 + rho) : blk * 32 + rho;
      glds16<0>((const char*)src + (size_t)grow * 1280, lane16, lds0 + rho * RP);
    }
    if (lane < 16) {
#pragma unroll
      for (int r = 0; r < 4; ++r) {
        const int rho = 4 * w + r;
        const int grow = MODE == 0 ? row_of(blk * 32 + rho) : blk * 32 + rho;
        glds16<1024>((const char*)src + (size_t)grow * 1280, lane16, lds0 + rho * RP);
      }
    }
  } else {
    const unsigned vo = (lane >> 2) * (FF_ * 2) + (lane & 3) * 16;
#pragma unroll
    for (int p = 0; p < 5; ++p)
      glds16<0>((const char*)src + blk * 64 + (size_t)(5 * w + p) * (16 * FF_ * 2), vo, lds0 + (5 * w + p) * 1088);
  }
  if constexpr (MODE == 3) {
    wait_bar<0>();
    const unsigned rb = lds0 + (4 * g + (i >> 2)) * RP + (i & 3) * 8 + w * 160;
#pragma unroll
    for (int q = 0; q < 5; ++q)
      wst((char*)dst + ((size_t)((5 * w + q) * K2S + blk) * 64 + lane) * 16, lds_tr(rb + q * 32, rb + q * 32 + 16 * RP));
    return;
  }
  const char* tb = (const char*)&g_FFT;
  constexpr int NC = MODE == 2 ? 10 : 5;
  bf16x8 ta[6], tc[NC];
  sfor<6>([&](auto uc) { constexpr int u = decltype(uc)::value; gld16<(u & 3) * 1024>(ta[u], tb + 40960 + (u >> 2) * 4096, lane16); });
  sfor<NC>([&](auto uc) {
    constexpr int u = decltype(uc)::value;
    gld16<(u & 3) * 1024>(tc[u], tb + (MODE == 2 ? (w >> 1) * 10240 : w * 5120) + (u >> 2) * 4096, lane16);
  });
  asm volatile("s_waitcnt vmcnt(%6)" : "+v"(ta[0]), "+v"(ta[1]), "+v"(ta[2]), "+v"(ta[3]), "+v"(ta[4]), "+v"(ta[5]) : "n"(NC) : "memory");
  if constexpr (MODE == 2) lgkm_bar();
  TS(1);
  if constexpr (MODE < 2) {
    f32x4 a[4][3];
#pragma unroll
    for (int r = 0; r < 4; ++r) {
      const unsigned rb = lds0 + (4 * w + r) * RP;
      fft_step_a(a[r], ta, rb + lane * 8, rb + 512 + lane * 8, rb + 1024 + (lane & 31) * 8);
    }
    const unsigned wb = lds0 + 4 * w * RP + g * 128 + i * 4;
#pragma unroll
    for (int r = 0; r < 4; ++r)
#pragma unroll
      for (int T = 0; T < 2; ++T) {
        *(LAS unsigned*)(uintptr_t)(wb + r * RP + T * 512) = pack2(a[r][T][0], a[r][T][1]);
        *(LAS unsigned*)(uintptr_t)(wb + r * RP + T * 512 + 64) = pack2(a[r][T][2], a[r][T][3]);
      }
    if (g < 2) {
#pragma unroll
      for (int r = 0; r < 4; ++r) {
        *(LAS unsigned*)(uintptr_t)(wb + r * RP + 1024) = pack2(a[r][2][0], a[r][2][1]);
        *(LAS unsigned*)(uintptr_t)(wb + r * RP + 1024 + 64) = pack2(a[r][2][2], a[r][2][3]);
      }
    }
  } else {
    const int j = w & 3, ft = w >> 2;
    const unsigned rb = lds0 + (4 * g + (i >> 2)) * 1088 + j * 256 + (i & 3) * 8 + ft * 32;
    const unsigned rt = lds0 + (32 + 4 * (g & 1) + (i >> 2)) * 1088 + j * 256 + (i & 3) * 8 + ft * 32;
    f32x4 a[4][3];
#pragma unroll
    for (int dd = 0; dd < 4; ++dd) fft_step_a(a[dd], ta, rb + dd * 64, rb + dd * 64 + 16 * 1088, rt + dd * 64);
    u32x4 u[3][2];
#pragma unroll
    for (int T = 0; T < 3; ++T)
#pragma unroll
      for (int dd = 0; dd < 4; ++dd) {
        u[T][0][dd] = pack2(a[dd][T][0], a[dd][T][1]);
        u[T][1][dd] = pack2(a[dd][T][2], a[dd][T][3]);
      }
    lgkm_bar();
    const unsigned wb = lds0 + (16 * ft + i) * RP + g * 128 + j * 16;
#pragma unroll
    for (int T = 0; T < 3; ++T)
      if (T < 2 || g < 2) {
        *(LAS u32x4*)(uintptr_t)(wb + T * 512) = u[T][0];
        *(LAS u32x4*)(uintptr_t)(wb + T * 512 + 64) = u[T][1];
      }
  }
  TS(2);
  lgkm_bar();
  asm volatile("s_waitcnt vmcnt(0)" : "+v"(tc[0]), "+v"(tc[1]), "+v"(tc[2]), "+v"(tc[3]), "+v"(tc[4])::"memory");
  TS(3);
  const unsigned db = lds0 + i * RP + g * 16;
  if constexpr (MODE == 2) {
    asm volatile("" : "+v"(tc[NC - 5]), "+v"(tc[NC - 4]), "+v"(tc[NC - 3]), "+v"(tc[NC - 2]), "+v"(tc[NC - 1]));
#pragma unroll
    for (int q = 0; q < 5; ++q) {
      const int G = 5 * (w >> 1) + q;
      const bf16x8 d = lds128(db + (w & 1) * 16 * RP + G * 64);
      f32x4 a = mfma16(tc[2 * q], d, f32x4{});
      const f32x4 b = mfma16(tc[2 * q + 1], d, f32x4{});
      if (q == 0 && w < 2 && g == 0) a[1] = 0.f;
      wst((char*)dst + ((size_t)((2 * blk + (w & 1)) * K1S + G) * 64 + lane) * 16, pack8(a, b));
    }
    return;
  }
  float sq[2] = {};
#pragma unroll
  for (int q = 0; q < 5; ++q) {
    const int u = 5 * w + q, G = u >> 1;
    const bf16x8 d0 = lds128(db + G * 64), d1 = lds128(db + 16 * RP + G * 64);
    if constexpr (MODE == 1) {
      const f32x4 a = mfma16(d0, tc[q], f32x4{}), b = mfma16(d1, tc[q], f32x4{});
      wst((char*)dst + ((size_t)(u * K2S + blk) * 64 + lane) * 16, pack8(a, b));
    } else {
      f32x4 a = mfma16(tc[q], d0, f32x4{}), b = mfma16(tc[q], d1, f32x4{});
      if constexpr (MODE == 0) {
        char* o = (char*)dst + (size_t)(2 * blk) * XP + u * 512 + lane * 8;
        wst(o, pack4(a));
        wst(o + XP, pack4(b));
      }
      if (q == 0 && w == 0 && g == 0) { a[1] = 0.f; b[1] = 0.f; }
      if constexpr (MODE == 0) {
        sq[0] += (a[0] * a[0] + a[1] * a[1]) + (a[2] * a[2] + a[3] * a[3]);
        sq[1] += (b[0] * b[0] + b[1] * b[1]) + (b[2] * b[2] + b[3] * b[3]);
      } else {
        char* o = (char*)dst + ((size_t)(2 * blk * K1S + G) * 64 + lane) * 16 + (u & 1) * 8;
        wst(o, pack4(a));
        wst(o + B1S, pack4(b));
      }
    }
  }
  if constexpr (MODE == 0) {
    float s = (g & 1) ? sq[1] : sq[0];
    s += __shfl_xor((g & 1) ? sq[0] : sq[1], 16);
    s += __shfl_xor(s, 32);
    if (g < 2) wst(part + w * S_ + blk * 32 + lane, s);
  }
}

DI void prefetch32k(const void* p, int blk) {
  u32x4 v[4];
#pragma unroll
  for (int q = 0; q < 4; ++q) gldv<0>(v[q], (const char*)p + (size_t)blk * 32768 + q * 8192, threadIdx.x * 16);
  asm volatile("s_waitcnt vmcnt(0)" : "+v"(v[0]), "+v"(v[1]), "+v"(v[2]), "+v"(v[3])::"memory");
}
#define K1_PF 120
constexpr int K1_X = 64, K1_WT = 160;
__global__ __launch_bounds__(512) void k1_prep(const u16* __restrict__ x, const u16* __restrict__ wr, const u16* __restrict__ wi,
                                              const u16* __restrict__ w1, const u16* __restrict__ w2, u16* __restrict__ X0T,
                                              float* __restrict__ part, unsigned* __restrict__ WT,
                                              const u16* __restrict__ out DBGARG) {
  TSDECL; TS(0);
  extern __shared__ __attribute__((aligned(16))) char smem[];
  const unsigned lds0 = (unsigned)(uintptr_t)(LAS char*)smem;
  const int tid = threadIdx.x;
  int b = blockIdx.x;
  if (b < K1_X) {
    fft640_block<0>(b, lds0, x, X0T, part TSPASS);
  } else if (b >= K1_X + K1_WT) {
    b -= K1_X + K1_WT;
    prefetch32k(b < 60 ? w1 : b < 120 ? w2 : out, b < 60 ? b : b < 120 ? b - 60 : b - 120);
  } else {
    b -= K1_X;
    if (b < 4) wst((unsigned*)(part + 16 * S_) + b * 512 + tid, 0u);
    const int kt = b % 10, ot = b / 10;
    const int kc = tid & 31, r = tid >> 5, e = r & 7;
    const int t0 = 16 * (ot >> 2) + (r >> 3) + 64 * (16 * (e >> 2) + 4 * (ot & 3) + (e & 3));
    const u16* pr = wr + t0 * 321 + kt * 32 + kc + 1;
    const u16* pi = wi + t0 * 321 + kt * 32 + kc + 1;
    unsigned v[8];
#pragma unroll
    for (int it = 0; it < 8; ++it) v[it] = (unsigned)pr[it * 642] | ((unsigned)pi[it * 642] << 16);
    const unsigned wa = lds0 + (kc * 132 + r) * 4;
#pragma unroll
    for (int it = 0; it < 8; ++it) *(LAS unsigned*)(uintptr_t)(wa + it * 64) = v[it];
    lgkm_bar();
#pragma unroll
    for (int it = 0; it < 2; ++it) {
      const int kc2 = r + 16 * it;
      const int c = (kt * 32 + kc2 + 1) % 320, rr = c % 40, j = c / 40;
      const int cp = (rr == 0 ? 0 : rr == 20 ? 8 : rr < 20 ? 16 * rr : 16 * (40 - rr) + 8) + j;
      wst(WT + cp * 2048 + ot * 128 + kc * 4, *(const LAS u32x4*)(uintptr_t)(lds0 + (kc2 * 132 + kc * 4) * 4));
    }
  }
  TSOUT;
}

struct Cx { float c, s; };
DI Cx cis(float rev) { return Cx{__builtin_amdgcn_cosf(rev), __builtin_amdgcn_sinf(rev)}; }
DI Cx cmul(Cx a, Cx b) { return Cx{a.c * b.c - a.s * b.s, a.c * b.s + a.s * b.c}; }
constexpr int C_CH = 576, C_PL = 8 * C_CH, C_OP = 32 * 272;
constexpr int C_MID = 2 * C_OP, C_HALF = C_MID + 2 * C_PL, C_LDS = 2 * C_HALF;
#define PDELAY 0
template <bool SECOND>
__global__ __launch_bounds__(512) void k2_cols(const u16* Xin, const float* __restrict__ part, const unsigned* __restrict__ WT,
                                              u16* Xout, const u16* __restrict__ wa, u16* __restrict__ Wa,
                                              const u16* __restrict__ wb, u16* __restrict__ Wb DBGARG) {
  TSDECL; TS(0);
  extern __shared__ __attribute__((aligned(16))) char smem[];
  if (blockIdx.x >= 160) {
    const unsigned l0 = (unsigned)(uintptr_t)(LAS char*)smem;
    const int b = blockIdx.x - 160;
    if (PDELAY) __builtin_amdgcn_s_sleep(PDELAY);
    if constexpr (SECOND) {
      fft640_block<3>(b, l0, wa, Wa, nullptr TSPASS);
    }
    else if (b < 48) fft640_block<2>(b, l0, wa, Wa, nullptr TSPASS);
    else fft640_block<1>(b - 48, l0, wb, Wb, nullptr TSPASS);
    TSOUT;
    return;
  }
  const int half = uni(threadIdx.x >> 8), c = 2 * blockIdx.x + half;
  const unsigned ldsb = (unsigned)(uintptr_t)(LAS char*)smem, lds0 = ldsb + half * C_HALF;
  const int tid = threadIdx.x & 255, lane = tid & 63, w = uni(tid >> 6), g = lane >> 4, i = lane & 15;
  const int T = threadIdx.x;
  f32x4 ps[8];
  u32x4 xin[2], wv[2], res[2];
  const char* xb = (const char*)Xin + blockIdx.x * 128;
  const unsigned vin = (T >> 2) * XP + (T & 3) * 32, vout = (T >> 3) * XP + (T & 7) * 16;
  gldv<0>(xin[0], xb, vin);
  gldv<16>(xin[1], xb, vin);
#pragma unroll
  for (int p = 0; p < 8; ++p) gldv<0>(ps[p], part + p * S_, T * 16);
  gldv<0>(wv[0], WT + (size_t)c * 2048, tid * 32);
  gldv<16>(wv[1], WT + (size_t)c * 2048, tid * 32);
  gldv<0>(res[0], xb, vout);
  gldv<0>(res[1], xb + 64 * XP, vout);
  __builtin_amdgcn_sched_barrier(0);
  bf16x8 f1r[2], f1i[2], f1n[2], f2r[2], f2i[2], f2n[2];
  const int k1c = 16 * w + i;
  const unsigned odd = (i & 1) ? 0x80008000u : 0u;
  {
    const Cx st = cis((float)k1c * (-1.f / 64.f));
    Cx v = cis((float)((g * k1c) & 7) * (-1.f / 8.f));
    v.c *= 0.125f; v.s *= 0.125f;
    f32x4 cr[2], ci[2];
#pragma unroll
    for (int j = 0; j < 8; ++j) {
      cr[j >> 2][j & 3] = v.c; ci[j >> 2][j & 3] = v.s;
      if (j < 7) v = cmul(v, st);
    }
    f1r[0] = pack8(cr[0], cr[1]);
    f1i[0] = pack8(ci[0], ci[1]);
    f1r[1] = __builtin_bit_cast(bf16x8, __builtin_bit_cast(u32x4, f1r[0]) ^ odd);
    f1i[1] = __builtin_bit_cast(bf16x8, __builtin_bit_cast(u32x4, f1i[0]) ^ odd);
    f1n[0] = neg8(f1i[0]);
    f1n[1] = neg8(f1i[1]);
  }
  {
    const Cx st = cis((float)i * (-1.f / 32.f));
    Cx v = cis((float)((g * i) & 7) * (-1.f / 8.f));
    v.c *= 0.17677669529663688f; v.s *= 0.17677669529663688f;
    f32x4 cr, ci;
#pragma unroll
    for (int j = 0; j < 4; ++j) {
      cr[j] = v.c; ci[j] = v.s;
      if (j < 3) v = cmul(v, st);
    }
    const u32x2 pr = pack4(cr), pi = pack4(ci);
    const u32x4 r0 = {pr[0], pr[1], pr[0] ^ odd, pr[1] ^ odd}, i0 = {pi[0], pi[1], pi[0] ^ odd, pi[1] ^ odd};
    f2r[0] = __builtin_bit_cast(bf16x8, r0);
    f2i[0] = __builtin_bit_cast(bf16x8, i0);
    f2r[1] = __builtin_bit_cast(bf16x8, r0 ^ 0x80000000u);
    f2i[1] = __builtin_bit_cast(bf16x8, i0 ^ 0x80000000u);
    f2n[0] = neg8(f2i[0]);
    f2n[1] = neg8(f2i[1]);
  }
  f32x4 twc[2], tws[2], tvc[2], tvs[2];
  {
    const Cx st = cis((float)k1c * (1.f / 2048.f)), jp = cis((float)k1c * (1.f / 128.f));
    Cx v = cis((float)(g * k1c) * (1.f / 512.f));
#pragma unroll
    for (int r = 0; r < 4; ++r) {
      const Cx u = cmul(v, jp);
      twc[0][r] = v.c; tws[0][r] = v.s; twc[1][r] = u.c; tws[1][r] = u.s;
      if (r < 3) v = cmul(v, st);
    }
#pragma unroll
    for (int mt = 0; mt < 2; ++mt) {
      const Cx s2 = cis((float)(16 * mt + i) * (1.f / 2048.f));
      Cx u = cis((float)((16 * mt + i) * (4 * w + g)) * (1.f / 512.f));
#pragma unroll
      for (int r = 0; r < 4; ++r) {
        tvc[mt][r] = u.c; tvs[mt][r] = u.s;
        if (r < 3) u = cmul(u, s2);
      }
    }
  }
  asm volatile("" : "+v"(f1r[0]), "+v"(f1r[1]), "+v"(f1i[0]), "+v"(f1i[1]), "+v"(f1n[0]), "+v"(f1n[1]), "+v"(f2r[0]), "+v"(f2r[1]),
               "+v"(f2i[0]), "+v"(f2i[1]), "+v"(f2n[0]), "+v"(f2n[1]));
  asm volatile("" : "+v"(twc[0]), "+v"(twc[1]), "+v"(tws[0]), "+v"(tws[1]), "+v"(tvc[0]), "+v"(tvc[1]), "+v"(tvs[0]), "+v"(tvs[1]));
  TS(1);
  asm volatile("s_waitcnt vmcnt(4)" : "+v"(ps[0]), "+v"(ps[1]), "+v"(ps[2]), "+v"(ps[3]), "+v"(ps[4]), "+v"(ps[5]), "+v"(ps[6]), "+v"(ps[7]),
               "+v"(xin[0]), "+v"(xin[1])::"memory");
  {
    f32x4 rs = ((ps[0] + ps[1]) + (ps[2] + ps[3])) + ((ps[4] + ps[5]) + (ps[6] + ps[7]));
#pragma unroll
    for (int e = 0; e < 4; ++e) rs[e] = rsqrtf(rs[e] * (1.f / D_) + EPS_);
    const unsigned ad = ldsb + ((T & 15) >> 1) * C_CH + (T >> 4) * 16 + (T & 1) * 8;
#pragma unroll
    for (int cc = 0; cc < 2; ++cc) {
      const unsigned x0 = xin[0][cc], x1 = xin[0][2 + cc], x2 = xin[1][cc], x3 = xin[1][2 + cc];
      const u32x2 re = {pack2(bflo(x0) * rs[0], bflo(x1) * rs[1]), pack2(bflo(x2) * rs[2], bflo(x3) * rs[3])};
      u32x2 im = {pack2(bfhi(x0) * rs[0], bfhi(x1) * rs[1]), pack2(bfhi(x2) * rs[2], bfhi(x3) * rs[3])};
      if (cc == 0 && blockIdx.x == 0) im = u32x2{0, 0};
      *(LAS u32x2*)(uintptr_t)(ad + cc * C_HALF) = re;
      *(LAS u32x2*)(uintptr_t)(ad + cc * C_HALF + C_PL) = im;
    }
  }
  asm volatile("s_waitcnt vmcnt(2) lgkmcnt(0)\n\ts_barrier" : "+v"(wv[0]), "+v"(wv[1])::"memory");
  TS(2);
  if constexpr (SECOND) {
    const int wv8 = uni(threadIdx.x >> 6), idx = blockIdx.x >> 3;
    const unsigned dump = ldsb + C_LDS + wv8 * 1024;
    if (idx * 8 + wv8 < 46) glds16<0>((const char*)&g_FFT + (46 + idx * 8 + wv8) * 1024, lane * 16, dump);
  }
  const unsigned rd = lds0 + g * C_CH + i * 16;
  {
    f32x4 ar[2] = {}, ai[2] = {};
#pragma unroll
    for (int mt = 0; mt < 2; ++mt)
#pragma unroll
      for (int ks = 0; ks < 2; ++ks) {
        const bf16x8 ur = lds128(rd + ks * 4 * C_CH + mt * 256), ui = lds128(rd + C_PL + ks * 4 * C_CH + mt * 256);
        ar[mt] = mfma16(ur, f1r[ks], ar[mt]);
        ai[mt] = mfma16(ur, f1i[ks], ai[mt]);
        ar[mt] = mfma16(ui, f1n[ks], ar[mt]);
        ai[mt] = mfma16(ui, f1r[ks], ai[mt]);
      }
    f32x4 br[2], bi[2];
#pragma unroll
    for (int mt = 0; mt < 2; ++mt) {
      br[mt] = ar[mt] * twc[mt] + ai[mt] * tws[mt];
      bi[mt] = ai[mt] * twc[mt] - ar[mt] * tws[mt];
    }
    bf16x8 pr = pack8(br[0], br[1]), pi = pack8(bi[0], bi[1]);
    f32x4 yr[2] = {}, yi[2] = {};
#pragma unroll
    for (int mt = 0; mt < 2; ++mt) {
      yr[mt] = mfma16(f2r[mt], pr, yr[mt]);
      yi[mt] = mfma16(f2i[mt], pr, yi[mt]);
      yr[mt] = mfma16(f2n[mt], pi, yr[mt]);
      yi[mt] = mfma16(f2r[mt], pi, yi[mt]);
    }
#pragma unroll
    for (int mt = 0; mt < 2; ++mt) {
      const u32x4 ww = wv[mt];
#pragma unroll
      for (int r = 0; r < 4; ++r) {
        const float wr_ = bflo(ww[r]), wi_ = bfhi(ww[r]);
        br[mt][r] = yr[mt][r] * wr_ - yi[mt][r] * wi_;
        bi[mt][r] = yr[mt][r] * wi_ + yi[mt][r] * wr_;
      }
    }
    pr = pack8(br[0], br[1]);
    pi = pack8(bi[0], bi[1]);
    f32x4 zr[2] = {}, zi[2] = {};
#pragma unroll
    for (int nt = 0; nt < 2; ++nt) {
      zr[nt] = mfma16(pr, f2r[nt], zr[nt]);
      zi[nt] = mfma16(pi, f2r[nt], zi[nt]);
      zr[nt] = mfma16(pi, f2i[nt], zr[nt]);
      zi[nt] = mfma16(pr, f2n[nt], zi[nt]);
    }
    const unsigned ad = lds0 + C_MID + (2 * w + (g >> 1)) * C_CH + i * 16 + (g & 1) * 8;
#pragma unroll
    for (int nt = 0; nt < 2; ++nt) {
      const f32x4 a = zr[nt] * tvc[nt] - zi[nt] * tvs[nt];
      const f32x4 b = zi[nt] * tvc[nt] + zr[nt] * tvs[nt];
      *(LAS u32x2*)(uintptr_t)(ad + nt * 256) = pack4(a);
      *(LAS u32x2*)(uintptr_t)(ad + C_PL + nt * 256) = pack4(b);
    }
  }
  lgkm_bar();
  TS(3);
  {
    const unsigned r2 = rd + C_MID;
    const unsigned ad = lds0 + i * 272 + w * 64 + g * 16;
#pragma unroll
    for (int nt = 0; nt < 2; ++nt) {
      f32x4 vr = {}, vi = {};
#pragma unroll
      for (int ks = 0; ks < 2; ++ks) {
        const bf16x8 qr = lds128(r2 + ks * 4 * C_CH + nt * 256), qi = lds128(r2 + C_PL + ks * 4 * C_CH + nt * 256);
        vr = mfma16(f1r[ks], qr, vr);
        vi = mfma16(f1r[ks], qi, vi);
        vr = mfma16(f1i[ks], qi, vr);
        vi = mfma16(f1n[ks], qr, vi);
      }
      *(LAS f32x4*)(uintptr_t)(ad + nt * 16 * 272) = vr;
      *(LAS f32x4*)(uintptr_t)(ad + C_OP + nt * 16 * 272) = vi;
    }
  }
  asm volatile("s_waitcnt vmcnt(0) lgkmcnt(0)\n\ts_barrier" : "+v"(res[0]), "+v"(res[1])::"memory");
  TS(4);
  const unsigned ro = ldsb + (T >> 5) * 272 + (T & 31) * 8;
#pragma unroll
  for (int hh = 0; hh < 2; ++hh) {
    f32x2 v[4];
#pragma unroll
    for (int p = 0; p < 4; ++p) v[p] = *(const LAS f32x2*)(uintptr_t)(ro + hh * 16 * 272 + (p >> 1) * C_HALF + (p & 1) * C_OP);
    if (blockIdx.x == 0) v[1] = f32x2{0.f, 0.f};
    const u32x4 r = res[hh];
    const u32x4 o = {pack2(bflo(r[0]) + v[0][0], bfhi(r[0]) + v[1][0]), pack2(bflo(r[1]) + v[2][0], bfhi(r[1]) + v[3][0]),
                     pack2(bflo(r[2]) + v[0][1], bfhi(r[2]) + v[1][1]), pack2(bflo(r[3]) + v[2][1], bfhi(r[3]) + v[3][1])};
    wst((char*)Xout + blockIdx.x * 128 + hh * 64 * XP + vout, o);
  }
  TSOUT;
}

DI bf16x8 lds_2x64(unsigned a) {
  const u32x2 lo = *(const LAS u32x2*)(uintptr_t)a, hi = *(const LAS u32x2*)(uintptr_t)(a + 512);
  return __builtin_bit_cast(bf16x8, u32x4{lo[0], lo[1], hi[0], hi[1]});
}
constexpr int G1_EX = 81920, G1_ST = G1_EX + 64 * 1024, G1_CN = G1_ST + 512, G1_IF = G1_CN + 64, IFP = 656, G1_LDS = G1_IF + 16 * IFP;
template <bool ARRIVE> DI void g1_sync(unsigned ca, int lane) {
  if (ARRIVE && lane == 0) __hip_atomic_fetch_add((LAS unsigned*)(uintptr_t)ca, 1u, __ATOMIC_RELAXED, __HIP_MEMORY_SCOPE_WORKGROUP);
  while (uni(*(volatile LAS unsigned*)(uintptr_t)ca) < 4) {}
  asm volatile("" ::: "memory");
}
template <int KM, int K0, int NS, int XH, int XI, int D, int A, int NX, class F>
DI void g1_body(f32x4 (&acc)[3][4], f32x4& dg, unsigned lds0, int lane, int w, const char* xb, const char* wb, const char* wb2,
                F&& extra TSREF) {
  constexpr int XBAR = XI + D;
  static_assert(XBAR < XH && XI < A, "");
  constexpr auto issued = [](int s) { return s >= A || s + 1 + D > NS ? NS : s + 1 + D; };
  const unsigned lane16 = lane * 16, xd = lds0 + (w * 20 + K0) * 1024;
  const int cx = w ^ (2 * KM);
  auto xissue = [&](auto sc) {
    constexpr int s = decltype(sc)::value;
    glds16<(s & 3) * 1024>(xb + K0 * 1024 + (s >> 2) * 4096, lane16, xd + (s >> 2) * 4096);
  };
  sfor<XH>(xissue);
  if (KM == 0 && threadIdx.x < 8) *(LAS unsigned*)(uintptr_t)(lds0 + G1_CN + threadIdx.x * 4) = 0;
  lgkm_bar();
  auto gsync = [&](int n) { g1_sync<true>(lds0 + G1_CN + (KM * 2 + n) * 4, lane); };
  bf16x8 wq[NS][3], xf[2][4];
  auto wissue = [&](auto sc) {
    constexpr int s = decltype(sc)::value;
    gld16<(s & 3) * 1024>(wq[s][0], wb + K0 * 1024 + (s >> 2) * 4096, lane16);
    gld16<(s & 3) * 1024>(wq[s][1], wb + B1S + K0 * 1024 + (s >> 2) * 4096, lane16);
    gld16<(s & 3) * 1024>(wq[s][2], wb2 + K0 * 1024 + (s >> 2) * 4096, lane16);
  };
  sfor<D>(wissue);
  const unsigned dcm = lane < 16 ? 0xffffu : 0xffffffffu;
  unsigned xa[4];
#pragma unroll
  for (int c = 0; c < 4; ++c) xa[c] = lds0 + ((c ^ cx) * 20 + K0) * 1024 + lane * 8;
  auto rd = [&](auto sc) {
    constexpr int s = decltype(sc)::value;
#pragma unroll
    for (int c = 0; c < 4; ++c) xf[s & 1][c] = lds_2x64(xa[c] + s * 1024);
  };
  TS(1);
  asm volatile("s_waitcnt vmcnt(%0)" ::"n"(3 * D) : "memory");
  gsync(0);
  TS(2);
  rd(std::integral_constant<int, 0>{});
  sfor<NS>([&](auto sc) {
    constexpr int s = decltype(sc)::value;
    constexpr int have = issued(s - 1), next = issued(s);
    constexpr int pend = 3 * (have - 1 - s) + (s > XI && s < XBAR ? NS - XH : 0) + (s > A ? NX : 0);
    if constexpr (s + 1 < NS && s != XBAR) rd(std::integral_constant<int, s + 1>{});
    __builtin_amdgcn_sched_barrier(0);
    qwaitv<pend, 3>(wq[s]);
    if constexpr (s == XBAR) {
      gsync(1);
      rd(std::integral_constant<int, s + 1>{});
      __builtin_amdgcn_sched_barrier(0);
    }
    TS(8 + s);
#pragma unroll
    for (int q = 0; q < 3; ++q)
#pragma unroll
      for (int c = 0; c < 4; ++c) acc[q][c] = mfma16(wq[s][q], xf[s & 1][c], acc[q][c]);
    u32x4 u = __builtin_bit_cast(u32x4, xf[s & 1][0]);
    if constexpr (K0 + s == 0) u[0] &= dcm;
    const bf16x8 v = __builtin_bit_cast(bf16x8, u);
    dg = mfma16(v, v, dg);
    __builtin_amdgcn_sched_barrier(0);
    if constexpr (s == XI) sfor<NS - XH>([&](auto kc) { xissue(std::integral_constant<int, XH + decltype(kc)::value>{}); });
    sfor<next - have>([&](auto kc) { wissue(std::integral_constant<int, have + decltype(kc)::value>{}); });
    if constexpr (s == A) extra();
    __builtin_amdgcn_sched_barrier(0);
  });
}
#define G1P0 0, 10, 5, 1, 3, 5
#define G1P1 10, 10, 5, 1, 3, 5
DI f32x4 silu4(f32x4 v) {
  f32x4 o;
#pragma unroll
  for (int r = 0; r < 4; ++r) o[r] = v[r] * __builtin_amdgcn_rcpf(1.f + __builtin_amdgcn_exp2f(v[r] * -1.4426950408889634f));
  return o;
}
template <int OFF, class T> DI void gld_sc1(T& dst, const void* sbase, unsigned voff) {
  if constexpr (sizeof(T) == 16)
    asm volatile("global_load_dwordx4 %0, %1, %2 offset:%3 sc1" : "=v"(dst) : "v"(voff), "s"(sbase), "n"(OFF) : "memory");
  else if constexpr (sizeof(T) == 8)
    asm volatile("global_load_dwordx2 %0, %1, %2 offset:%3 sc1" : "=v"(dst) : "v"(voff), "s"(sbase), "n"(OFF) : "memory");
  else
    asm volatile("global_load_dword %0, %1, %2 offset:%3 sc1" : "=v"(dst) : "v"(voff), "s"(sbase), "n"(OFF) : "memory");
}
constexpr int G2_GO = G1_CN + 32;
#define XMAP 0
#define W2A 2
template <bool LAST, int km>
DI void mlp_body(const u16* __restrict__ XT, const u16* __restrict__ W1F, u16* H, const u16* __restrict__ W2, u16* R, u16* __restrict__ Y,
                 float* __restrict__ part, unsigned* flags DBGARG) {
  TSDECL; TS(0);
  extern __shared__ __attribute__((aligned(16))) char smem[];
  const unsigned lds0 = (unsigned)(uintptr_t)(LAS char*)smem;
  const int tid = threadIdx.x, lane = tid & 63, w = uni(tid >> 6) & 3, wave = w + 4 * km, g = lane >> 4, i = lane & 15;
  const int b = blockIdx.x, xcd = b & 7, j = b >> 3;
  const int mt = (xcd >> 1) * 8 + (j & 7), nt = (xcd & 1) * 4 + (j >> 3);
  const int e = w & 1, hf = w >> 1, cx = w ^ (2 * km);
  const unsigned lane16 = lane * 16;
  unsigned* fl = flags + mt * 32;
  const char* xb = (const char*)XT + (size_t)(mt * 4 + w) * XP;
  const char* a2 = (const char*)W2 + (size_t)nt * 5 * B2S + wave * 6144;
  bf16x8 wr[4][5];
  for (int ft = nt;;) {
    if (km == 0 && tid == 0) wst(fl + ft, 1u);
    const char* wb = (const char*)W1F + (size_t)(ft * 12 + hf * 6 + 2 * e) * B1S;
    f32x4 acc[3][4] = {};
    f32x4 dg = {};
    bf16x8 tc[5], ta[4];
    auto tabs = [&]() {
      if constexpr (LAST) {
        const char* tb = (const char*)&g_FFT + 46 * 1024;
        sfor<5>([&](auto uc) { constexpr int u = decltype(uc)::value; gld16<(u & 3) * 1024>(tc[u], tb + ((ft & 1) * 20 + w * 5 + (u & 4)) * 1024, lane16); });
        gld16<0>(ta[0], tb + (40 + 2 * hf) * 1024, lane16);
        gld16<1024>(ta[1], tb + (40 + 2 * hf) * 1024, lane16);
        gld16<0>(ta[2], tb + 44 * 1024, lane16);
        gld16<1024>(ta[3], tb + 44 * 1024, lane16);
      }
    };
    if constexpr (km == 0) {
      g1_body<0, G1P0, LAST ? 9 : 0>(acc, dg, lds0, lane, w, xb, wb, wb + (4 - e) * B1S, tabs TSPASS);
      if constexpr (LAST)
        asm volatile("s_waitcnt vmcnt(0)" : "+v"(tc[0]), "+v"(tc[1]), "+v"(tc[2]), "+v"(tc[3]), "+v"(tc[4]), "+v"(ta[0]), "+v"(ta[1]),
                     "+v"(ta[2]), "+v"(ta[3])::"memory");
    } else g1_body<1, G1P1, 0>(acc, dg, lds0, lane, w, xb, wb, wb + (4 - e) * B1S, [] {} TSPASS);
    TS(3);
    auto w2issue = [&](auto sc) {
      constexpr int s = decltype(sc)::value;
#pragma unroll
      for (int q = 0; q < 5; ++q) gld16<s * 1024>(wr[s][q], a2 + q * B2S, lane16);
    };
    if constexpr (km == 0) sfor<4>(w2issue);
    {
      const unsigned ex = lds0 + G1_EX + wave * 8192 + lane16;
#pragma unroll
      for (int c = 2; c < 4; ++c)
#pragma unroll
        for (int q = 0; q < 2; ++q) *(LAS f32x4*)(uintptr_t)(ex + ((c - 2) * 2 + q) * 1024) = acc[q][c];
#pragma unroll
      for (int c = 1; c < 4; ++c) *(LAS f32x4*)(uintptr_t)(ex + (4 + c) * 1024) = acc[2][c];
      const int q4 = i >> 2, p4 = i & 3;
      if (g == q4) {
        const float d0 = dg[0], d1 = dg[1], d2 = dg[2], d3 = dg[3];
        *(LAS float*)(uintptr_t)(lds0 + G1_ST + km * 256 + (cx * 16 + i) * 4) = p4 == 0 ? d0 : p4 == 1 ? d1 : p4 == 2 ? d2 : d3;
      }
    }
    if constexpr (LAST) {
      if constexpr (km == 0) {
        g1_sync<false>(lds0 + G1_CN + 12, lane);
        const unsigned xs = lds0 + ((ft >> 1) * 20 + w * 5) * 1024 + lane * 8, mw = lds0 + G1_IF + i * IFP + g * 160 + w * 20;
#pragma unroll
        for (int q = 0; q < 5; ++q) {
          const f32x4 d = mfma16(tc[q], lds_2x64(xs + q * 1024), f32x4{});
          *(LAS unsigned*)(uintptr_t)(mw + q * 4) = pack2(d[0], d[1]);
          *(LAS unsigned*)(uintptr_t)(mw + q * 4 + 80) = pack2(d[2], d[3]);
        }
        asm volatile("s_waitcnt lgkmcnt(0)" ::: "memory");
        g1_sync<true>(lds0 + G1_CN + 16, lane);
        const unsigned mr = lds0 + G1_IF + i * IFP + e * 320 + g * 16;
        bf16x8 b0[4], b1[4];
#pragma unroll
        for (int dd = 0; dd < 4; ++dd) { b0[dd] = lds128(mr + dd * 80); b1[dd] = lds128(mr + dd * 80 + 16); }
        char* yo = (char*)R + (size_t)(mt * 4 + (ft >> 1)) * XP + (16 * g + 2 * (ft & 1) + e) * 128 + i * 8;
        auto unit = [&](bf16x8 t0, bf16x8 t1, int T, bool st) {
          f32x4 o[4];
#pragma unroll
          for (int dd = 0; dd < 4; ++dd) o[dd] = mfma16(t1, b1[dd], mfma16(t0, b0[dd], f32x4{}));
          if (st) {
#pragma unroll
            for (int r = 0; r < 4; ++r) wst(yo + T * 8192 + r * 512, pack4(f32x4{o[0][r], o[1][r], o[2][r], o[3][r]}));
          }
        };
        unit(ta[0], ta[1], hf, true);
        if (hf == 0) unit(ta[2], ta[3], 2, g < 2);
      }
    }
    lgkm_bar();
    TS(4);
    if constexpr (km != 0) sfor<W2A>(w2issue);
    const unsigned ep = lds0 + G1_EX + (wave ^ 4) * 8192 + lane16, en = lds0 + G1_EX + (wave ^ 1) * 8192 + lane16,
                   eo = lds0 + G1_EX + (wave ^ 5) * 8192 + lane16;
    float rstd[2];
#pragma unroll
    for (int c = 0; c < 2; ++c) {
      const unsigned sa = lds0 + G1_ST + ((c ^ cx) * 16 + i) * 4;
      rstd[c] = rsqrtf((*(const LAS float*)(uintptr_t)sa + *(const LAS float*)(uintptr_t)(sa + 256)) * (1.f / D_) + EPS_);
    }
    char* hb = (char*)H + (size_t)(mt * 4 * K2S + ft * 6 + hf * 3) * 1024 + lane16;
#pragma unroll
    for (int c = 0; c < 2; ++c) {
      const u32x2 h0 = pack4(silu4((acc[0][c] + *(const LAS f32x4*)(uintptr_t)(ep + (c * 2) * 1024)) * rstd[c]));
      const u32x2 h1 = pack4(silu4((acc[1][c] + *(const LAS f32x4*)(uintptr_t)(ep + (c * 2 + 1) * 1024)) * rstd[c]));
      wst(hb + ((c ^ cx) * K2S + e) * 1024, u32x4{h0[0], h0[1], h1[0], h1[1]});
    }
    {
      const u32x2 h0 = pack4(silu4((acc[2][0] + *(const LAS f32x4*)(uintptr_t)(ep + 6 * 1024)) * rstd[0]));
      const u32x2 h1 = pack4(silu4((*(const LAS f32x4*)(uintptr_t)(en + 5 * 1024) + *(const LAS f32x4*)(uintptr_t)(eo + 7 * 1024)) * rstd[0]));
      wst(hb + (cx * K2S + 2) * 1024, e ? u32x4{h1[0], h1[1], h0[0], h0[1]} : u32x4{h0[0], h0[1], h1[0], h1[1]});
    }
#pragma unroll
    for (int s = 0; s < (km ? W2A : 4); ++s)
      asm volatile("s_waitcnt vmcnt(0)" : "+v"(wr[s][0]), "+v"(wr[s][1]), "+v"(wr[s][2]), "+v"(wr[s][3]), "+v"(wr[s][4])::"memory");
    asm volatile("s_waitcnt vmcnt(0)" ::: "memory");
    lgkm_bar();
    TS(5);
    if (km == 0 && tid == 0) wst(fl + 8 + ft, 1u);
    if constexpr (km != 0) sfor<4 - W2A>([&](auto sc) { w2issue(std::integral_constant<int, W2A + decltype(sc)::value>{}); });
#pragma unroll
    for (int q = 0; q < 5; ++q) {
      glds16<0>(a2 + q * B2S + 4096, lane16, lds0 + wave * 10240 + q * 2048);
      glds16<1024>(a2 + q * B2S + 4096, lane16, lds0 + wave * 10240 + q * 2048);
    }
    if (km == 0 && wave == 0) {
      int go = -1;
      for (int it = 0; it < (1 << 20); ++it) {
        unsigned v;
        gld_sc1<0>(v, fl, i * 4);
        asm volatile("s_waitcnt vmcnt(0)" : "+v"(v)::"memory");
        const unsigned m = (unsigned)__builtin_amdgcn_ballot_w64(v != 0) & 0xffffu;
        if ((m >> 8) == 0xffu) break;
        if ((m & 0xffu) != 0xffu) { go = __builtin_ctz(~m); break; }
      }
      if (lane == 0) *(LAS int*)(uintptr_t)(lds0 + G2_GO) = go;
    }
    lgkm_bar();
    const int go = uni(*(volatile LAS int*)(uintptr_t)(lds0 + G2_GO));
    if (go < 0) break;
    asm volatile("s_waitcnt vmcnt(0)" ::: "memory");
    lgkm_bar();
    ft = go;
  }
  TS(6);
  const int c2 = w;
  const char* b2 = (const char*)H + (size_t)mt * 4 * B2S + wave * 6144;
  f32x4 acc[5][4] = {};
  bf16x8 bf[4][4], af[5];
  u32x2 res[5];
  auto hissue = [&](auto sc) {
    constexpr int s = decltype(sc)::value;
#pragma unroll
    for (int c = 0; c < 4; ++c) gld_sc1<(s & 3) * 1024>(bf[s & 3][c], b2 + (size_t)(c ^ c2) * B2S + (s >> 2) * 4096, lane16);
  };
  sfor<4>(hissue);
  sfor<6>([&](auto sc) {
    constexpr int s = decltype(sc)::value;
    constexpr int pend = s <= 2 ? 12 : s == 3 ? 13 : s == 4 ? 9 : 5;
    bf16x8(&bb)[4] = bf[s & 3];
    asm volatile("s_waitcnt vmcnt(%4)" : "+v"(bb[0]), "+v"(bb[1]), "+v"(bb[2]), "+v"(bb[3]) : "n"(pend) : "memory");
    if constexpr (s < 4) asm volatile("" : "+v"(wr[s][0]), "+v"(wr[s][1]), "+v"(wr[s][2]), "+v"(wr[s][3]), "+v"(wr[s][4]));
    TS(20 + s);
    if constexpr (s >= 4) {
#pragma unroll
      for (int q = 0; q < 5; ++q) af[q] = lds128(lds0 + wave * 10240 + q * 2048 + (s - 4) * 1024 + lane16);
    }
#pragma unroll
    for (int q = 0; q < 5; ++q)
#pragma unroll
      for (int c = 0; c < 4; ++c) acc[q][c] = mfma16(s < 4 ? wr[s & 3][q] : af[q], bb[c], acc[q][c]);
    __builtin_amdgcn_sched_barrier(0);
    if constexpr (s < 2) hissue(std::integral_constant<int, s + 4>{});
    if constexpr (s == 2) {
#pragma unroll
      for (int q = 0; q < 5; ++q) gld_sc1<0>(res[q], (const char*)R + (size_t)(mt * 4 + w) * XP + (nt * 5 + q) * 512, lane * 8);
    }
    __builtin_amdgcn_sched_barrier(0);
  });
  TS(30);
  auto ulo = [&](int v) { return lds0 + v * 10240 + lane16; };
  auto uhi = [&](int v) { return lds0 + 81920 - 10240 + (v < 4 ? v * 10240 : v * 5120 + 20480) + lane16; };
  asm volatile("s_waitcnt lgkmcnt(0)" ::: "memory");
#pragma unroll
  for (int c = 1; c < 4; ++c)
#pragma unroll
    for (int q = 0; q < 5; ++q) *(LAS f32x4*)(uintptr_t)((c < 3 ? ulo(wave) : uhi(wave)) + ((c - 1) * 5 + q) * 1024) = acc[q][c];
  auto gather = [&](int q) {
    f32x4 u[3];
#pragma unroll
    for (int c = 1; c < 4; ++c) u[c - 1] = *(const LAS f32x4*)(uintptr_t)((c < 3 ? ulo(c ^ wave) : uhi(c ^ wave)) + ((c - 1) * 5 + q) * 1024);
    return (acc[q][0] + u[0]) + (u[1] + u[2]);
  };
  if constexpr (km == 0) {
    asm volatile("s_waitcnt lgkmcnt(0)" ::: "memory");
    g1_sync<true>(lds0 + G1_CN + 20, lane);
#pragma unroll
    for (int q = 0; q < 5; ++q) *(LAS f32x4*)(uintptr_t)(uhi(w) + (15 + q) * 1024) = gather(q);
    asm volatile("s_waitcnt vmcnt(0)" ::"v"(res[0]), "v"(res[1]), "v"(res[2]), "v"(res[3]), "v"(res[4]) : "memory");
    lgkm_bar();
    TSOUT;
    return;
  } else {
  f32x4 v[5];
  lgkm_bar();
  asm volatile("s_waitcnt vmcnt(0)" : "+v"(res[0]), "+v"(res[1]), "+v"(res[2]), "+v"(res[3]), "+v"(res[4])::"memory");
#pragma unroll
  for (int q = 0; q < 5; ++q) v[q] = gather(q) + (*(const LAS f32x4*)(uintptr_t)(uhi(w) + (15 + q) * 1024) + unpack4(res[q]));
  TS(31);
  if constexpr (!LAST) {
    float sq = 0.f;
    char* o = (char*)Y + (size_t)(mt * 4 + w) * XP + nt * 2560 + lane * 8;
#pragma unroll
    for (int q = 0; q < 5; ++q) {
      f32x4 u = v[q];
      wst(o + q * 512, pack4(u));
      if (q == 0 && nt == 0 && g == 0) u[1] = 0.f;
      sq += (u[0] * u[0] + u[1] * u[1]) + (u[2] * u[2] + u[3] * u[3]);
    }
    sq += __shfl_xor(sq, 16);
    sq += __shfl_xor(sq, 32);
    if (g == 0) wst(part + nt * S_ + mt * 64 + w * 16 + i, sq);
  } else {
    u16* o = Y + (size_t)row_of(mt * 64 + w * 16 + i) * D_ + nt * 80;
#pragma unroll
    for (int q = 0; q < 4; q += 2) {
      const u32x2 p0 = pack4(v[q]), p1 = pack4(v[q + 1]);
      const auto s0 = __builtin_amdgcn_permlane16_swap(p0[0], p1[0], false, false);
      const auto s1 = __builtin_amdgcn_permlane16_swap(p0[1], p1[1], false, false);
      wst(o + (q + (g & 1)) * 16 + (g >> 1) * 8, u32x4{s0[0], s1[0], s0[1], s1[1]});
    }
    wst(o + 64 + g * 4, pack4(v[4]));
  }
  TSOUT;
  }
}
template <bool LAST>
__global__ __launch_bounds__(512) void k3_mlp(const u16* __restrict__ XT, const u16* __restrict__ W1F, u16* H, const u16* __restrict__ W2,
                                             u16* R, u16* __restrict__ Y, float* __restrict__ part, unsigned* flags DBGARG) {
  if (uni(threadIdx.x) < 256) mlp_body<LAST, 0>(XT, W1F, H, W2, R, Y, part, flags DBGFWD);
  else mlp_body<LAST, 1>(XT, W1F, H, W2, R, Y, part, flags DBGFWD);
}

extern "C" void kernel_launch(void* const* d_in, const int* in_sizes, int n_in, void* d_out, int out_size, void* d_ws, size_t ws_size,
                              hipStream_t stream) {
  const u16* x = (const u16*)d_in[0];
  const u16* wr = (const u16*)d_in[1];
  const u16* wi = (const u16*)d_in[2];
  const u16* w1 = (const u16*)d_in[4];
  const u16* w2 = (const u16*)d_in[5];
  char* ws = (char*)d_ws;
  constexpr size_t XB = (size_t)XP * 128;
  u16* X0T = (u16*)(ws + 0 * XB);
  u16* X1T = (u16*)(ws + 1 * XB);
  u16* X2T = (u16*)(ws + 2 * XB);
  u16* X3T = (u16*)(ws + 3 * XB);
  u16* Y3 = (u16*)(ws + 4 * XB);
  unsigned* WT = (unsigned*)(ws + 5 * XB);
  u16* W1F = (u16*)(ws + 6 * XB);
  u16* W2F = (u16*)(ws + 7 * XB);
  u16* W2P = (u16*)(ws + 8 * XB);
  u16* H = (u16*)(ws + 9 * XB);
  u16* H2 = (u16*)(ws + 13 * XB);
  float* part0 = (float*)(ws + 12 * XB);
  float* part = part0 + 8 * S_;

  unsigned* flags = (unsigned*)(part + 8 * S_);
  (void)hipFuncSetAttribute((const void*)k3_mlp<false>, hipFuncAttributeMaxDynamicSharedMemorySize, G1_LDS);
  (void)hipFuncSetAttribute((const void*)k3_mlp<true>, hipFuncAttributeMaxDynamicSharedMemorySize, G1_LDS);

  k1_prep<<<K1_X + K1_WT + K1_PF + PFO, 512, 32 * RP, stream>>>(x, wr, wi, w1, w2, X0T, part0, WT, (const u16*)d_out DBGPASS(0));
  k2_cols<false><<<160 + 96, 512, C_LDS, stream>>>(X0T, part0, WT, X1T, w1, W1F, w2, W2F DBGPASS(1));
  k3_mlp<false><<<256, 512, G1_LDS, stream>>>(X1T, W1F, H, W2F, X1T, X2T, part, flags DBGPASS(2));
  k2_cols<true><<<160 + 48, 512, C_LDS + 8192, stream>>>(X2T, part, WT, X3T, w2, W2P, w2, W2P DBGPASS(3));
  k3_mlp<true><<<256, 512, G1_LDS, stream>>>(X3T, W1F, H2, W2P, Y3, (u16*)d_out, part, flags + 1024 DBGPASS(4));
}
```

```cpp
#include <hip/hip_runtime.h>
#include <hip/hip_bf16.h>
#include <stdint.h>
#include <utility>

typedef __bf16 bf16x8 __attribute__((ext_vector_type(8)));
typedef __bf16 bf16x4 __attribute__((ext_vector_type(4)));
typedef __bf16 bf16x2 __attribute__((ext_vector_type(2)));
typedef short s16x4 __attribute__((ext_vector_type(4)));
typedef short s16x8 __attribute__((ext_vector_type(8)));
typedef float f32x4 __attribute__((ext_vector_type(4)));
typedef float f32x2 __attribute__((ext_vector_type(2)));
typedef unsigned u32x4 __attribute__((ext_vector_type(4)));
typedef unsigned u32x2 __attribute__((ext_vector_type(2)));
typedef unsigned short u16;

#define DI __device__ __forceinline__
#define LAS __attribute__((address_space(3)))

#define DBGARG
#define DBGFWD
#define TSDECL
#define TSREF
#define TSPASS
#define TS(I)
#define TSOUT
#define DBGPASS(K)
constexpr int S_ = 2048, D_ = 640, FF_ = 1536;
constexpr int XP = 161 * 128;
constexpr float EPS_ = 1e-5f;
#define PFO 80
#define PADW 1
constexpr int K1S = 20 + PADW, K2S = 48 + PADW, B1S = K1S * 1024, B2S = K2S * 1024;

namespace tab {
constexpr double kPi = 3.14159265358979323846264338327950288;
constexpr double ccos(double x) {
  double x2 = x * x, term = 1.0, sum = 1.0;
  for (int n = 1; n <= 24; ++n) { term *= -x2 / (double)((2 * n - 1) * (2 * n)); sum += term; }
  return sum;
}
constexpr uint16_t tobf(double v) {
  float f = (float)v;
  uint32_t u = __builtin_bit_cast(uint32_t, f);
  u += 0x7fffu + ((u >> 16) & 1u);
  return (uint16_t)(u >> 16);
}
struct CosD { double c[640]; };
constexpr CosD mkcosd() {
  CosD t{};
  for (int j = 0; j < 640; ++j) {
    int jj = j <= 320 ? j : 640 - j;
    t.c[j] = (jj == 160) ? 0.0 : ccos(2.0 * kPi * jj / 640.0);
  }
  return t;
}
constexpr CosD kCd = mkcosd();
constexpr double cs(int n) { return kCd.c[n % 640]; }
constexpr double sn(int n) { return kCd.c[(n + 480) % 640]; }
constexpr double kS2 = 0.05590169943749474241, kS1 = 0.03952847075210474165;
constexpr int bin_of(int cp) {
  int G = cp >> 4, t = (cp >> 3) & 1, j = cp & 7;
  return (t ? (G ? 40 - G : 20) : G) + 40 * j;
}
constexpr double aval(int r, int d1) {
  if (r >= 40 || d1 >= 40) return 0.0;
  int G = r >> 1, p = r & 1;
  if (G == 0) return (p && (d1 & 1)) ? -0.25 : 0.25;
  return p ? -0.25 * sn(16 * d1 * G) : 0.25 * cs(16 * d1 * G);
}
constexpr double cval(int G, int t, int o, int d2, int p) {
  int j = o >> 1, po = o & 1, c = bin_of(16 * G + 8 * t + j);
  if (G == 0) {
    if (p != t) return 0.0;
    if (c == 0) return (po == 0 && (d2 & 1)) ? -4.0 * kS1 : 4.0 * kS1;
    return 4.0 * kS2 * (po ? -sn(d2 * c) : cs(d2 * c));
  }
  double v = po == 0 ? (p == 0 ? cs(d2 * c) : sn(d2 * c)) : (p == 0 ? -sn(d2 * c) : cs(d2 * c));
  if (t && p) v = -v;
  return 4.0 * kS2 * v;
}
struct Fft { uint16_t c[40][64][8]; uint16_t a[6][64][8]; uint16_t ic[40][64][8]; uint16_t ia[6][64][8]; };
constexpr Fft mkfft() {
  Fft f{};
  for (int l = 0; l < 64; ++l) {
    int g = l >> 4, i = l & 15;
    for (int u = 0; u < 40; ++u)
      for (int e = 0; e < 8; ++e) f.c[u][l][e] = tobf(cval(u >> 1, u & 1, i, 4 * g + (e >> 1), e & 1));
    for (int u = 0; u < 40; ++u)
      for (int e = 0; e < 8; ++e) f.ic[u][l][e] = tobf(cval(u % 20, e >> 2, 4 * g + (e & 3), 8 * (u / 20) + (i >> 1), i & 1));
    for (int T = 0; T < 3; ++T) {
      for (int e = 0; e < 8; ++e) f.a[T][l][e] = tobf(aval(16 * T + i, 16 * (e >> 2) + 4 * g + (e & 3)));
      for (int e = 4; e < 8; ++e) f.a[3 + T][l][e] = tobf(aval(16 * T + i, 28 + 4 * g + e));
      for (int e = 0; e < 8; ++e) f.ia[2 * T][l][e] = tobf(aval(8 * g + e, 16 * T + i));
      for (int e = 0; e < 8; ++e) f.ia[2 * T + 1][l][e] = g == 3 ? tobf(aval(32 + e, 16 * T + i)) : 0;
    }
  }
  return f;
}
}
#if defined(__HIP_DEVICE_COMPILE__)
static __device__ const __attribute__((aligned(1024))) tab::Fft g_FFT = tab::mkfft();
#else
static __device__ const __attribute__((aligned(1024))) tab::Fft g_FFT = {};
#endif

template <int... I, class F> DI void sfor_impl(std::integer_sequence<int, I...>, F&& f) {
  (f(std::integral_constant<int, I>{}), ...);
}
template <int N, class F> DI void sfor(F&& f) { sfor_impl(std::make_integer_sequence<int, N>{}, f); }

DI f32x4 mfma16(bf16x8 a, bf16x8 b, f32x4 c) { return __builtin_amdgcn_mfma_f32_16x16x32_bf16(a, b, c, 0, 0, 0); }

template <int OFF> DI void glds16(const void* sbase, unsigned voff, unsigned lds_dst) {
  unsigned keep;
  asm volatile("s_mov_b32 %0, m0\n\ts_mov_b32 m0, %3\n\ts_nop 0\n\tglobal_load_lds_dwordx4 %1, %2 offset:%4\n\ts_mov_b32 m0, %0"
               : "=&s"(keep) : "v"(voff), "s"(sbase), "s"(lds_dst), "n"(OFF) : "memory");
}
#define PRE_ 3
constexpr int last_issued(int b, int NS, int T) {
  int l = (PRE_ < NS ? PRE_ : NS) - 1;
  for (int k = 0; k < b; ++k) {
    int n = l + 2;
    if (n > k - 1 + NS) n = k - 1 + NS;
    if (n > l) l = n;
  }
  return l < T - 1 ? l : T - 1;
}
template <int N> DI void wait_bar() {
  asm volatile("s_waitcnt vmcnt(%0) lgkmcnt(0)\n\ts_barrier" ::"n"(N) : "memory");
}
DI void lgkm_bar() { asm volatile("s_waitcnt lgkmcnt(0)\n\ts_barrier" ::: "memory"); }
DI bf16x8 lds128(unsigned addr) { return *(const LAS bf16x8*)(uintptr_t)addr; }
DI bf16x8 lds_tr(unsigned a0, unsigned a1) {
  s16x4 lo = __builtin_amdgcn_ds_read_tr16_b64_v4i16((LAS s16x4*)(uintptr_t)a0);
  s16x4 hi = __builtin_amdgcn_ds_read_tr16_b64_v4i16((LAS s16x4*)(uintptr_t)a1);
  s16x8 v = __builtin_shufflevector(lo, hi, 0, 1, 2, 3, 4, 5, 6, 7);
  return __builtin_bit_cast(bf16x8, v);
}
DI unsigned pack2(float a, float b) {
  f32x2 f = {a, b};
  return __builtin_bit_cast(unsigned, __builtin_convertvector(f, bf16x2));
}
DI u32x2 pack4(f32x4 v) { return u32x2{pack2(v[0], v[1]), pack2(v[2], v[3])}; }
DI bf16x8 pack8(f32x4 a, f32x4 b) {
  u32x4 u = {pack2(a[0], a[1]), pack2(a[2], a[3]), pack2(b[0], b[1]), pack2(b[2], b[3])};
  return __builtin_bit_cast(bf16x8, u);
}
DI float bflo(unsigned u) { return __builtin_bit_cast(float, u << 16); }
DI float bfhi(unsigned u) { return __builtin_bit_cast(float, u & 0xffff0000u); }
DI f32x4 unpack4(u32x2 u) { return f32x4{bflo(u[0]), bfhi(u[0]), bflo(u[1]), bfhi(u[1])}; }
DI bf16x8 neg8(bf16x8 v) {
  u32x4 u = __builtin_bit_cast(u32x4, v);
  u ^= 0x80008000u;
  return __builtin_bit_cast(bf16x8, u);
}
DI int row_of(int m) { return 32 * (m & 63) + (m >> 6); }
DI int uni(int v) { return __builtin_amdgcn_readfirstlane(v); }
template <class T> DI void wst(void* p, T v) {
  static_assert(sizeof(T) == 16 || sizeof(T) == 8 || sizeof(T) == 4, "");
  if constexpr (sizeof(T) == 16) asm volatile("global_store_dwordx4 %0, %1, off sc1\n\ts_nop 1" ::"v"(p), "v"(v) : "memory");
  else if constexpr (sizeof(T) == 8) asm volatile("global_store_dwordx2 %0, %1, off sc1" ::"v"(p), "v"(v) : "memory");
  else asm volatile("global_store_dword %0, %1, off sc1" ::"v"(p), "v"(v) : "memory");
}

template <int OFF> DI void gld16(bf16x8& dst, const void* sbase, unsigned voff) {
  asm volatile("global_load_dwordx4 %0, %1, %2 offset:%3" : "=v"(dst) : "v"(voff), "s"(sbase), "n"(OFF) : "memory");
}
template <int N> DI void qwait(bf16x8& a, bf16x8& b) { asm volatile("s_waitcnt vmcnt(%2)" : "+v"(a), "+v"(b) : "n"(N) : "memory"); }
template <int N> DI void qwait(bf16x8& a, bf16x8& b, bf16x8& c) {
  asm volatile("s_waitcnt vmcnt(%3)" : "+v"(a), "+v"(b), "+v"(c) : "n"(N) : "memory");
}
template <int N, int NT> DI void qwaitv(bf16x8 (&f)[NT]) {
  if constexpr (NT == 2) qwait<N>(f[0], f[1]); else qwait<N>(f[0], f[1], f[2]);
}

template <int NS, int T1, int PK1, int T2, int PK2>
DI void p_loader(unsigned lds0, int w, int lane, const char* p1base, unsigned p1v0, unsigned p1v1, unsigned p1step,
                 const char* p2base, unsigned p2v0, unsigned p2v1, unsigned p2step) {
  constexpr int T = T1 + T2;
  auto issue = [&](auto tc) {
    constexpr int t = decltype(tc)::value;
    const unsigned slot = lds0 + (t % NS) * 8192;
    constexpr bool ph1 = t < T1;
    constexpr int tt = ph1 ? t : t - T1;
    constexpr int PK = ph1 ? PK1 : PK2;
    const char* pb = (ph1 ? p1base : p2base) + (size_t)tt * (ph1 ? p1step : p2step);
    if constexpr (PK == 1) {
      glds16<0>(pb, ph1 ? p1v0 : p2v0, slot + w * 2048);
      glds16<1024>(pb, ph1 ? p1v0 : p2v0, slot + w * 2048);
    } else {
      glds16<0>(pb, ph1 ? p1v0 : p2v0, slot + w * 2048);
      glds16<0>(pb, ph1 ? p1v1 : p2v1, slot + w * 2048 + 1024);
    }
  };
  sfor<last_issued(0, NS, T) + 1>(issue);
  sfor<T>([&](auto tc) {
    constexpr int b = decltype(tc)::value;
    constexpr int last = last_issued(b, NS, T), nxt = last_issued(b + 1, NS, T);
    wait_bar<2 * (last - b)>();
    sfor<nxt - last>([&](auto kc) { issue(std::integral_constant<int, last + 1 + decltype(kc)::value>{}); });
  });
}
constexpr int RP = 1296;
template <int OFF, class T> DI void gldv(T& dst, const void* sbase, unsigned voff) {
  asm volatile("global_load_dwordx4 %0, %1, %2 offset:%3" : "=v"(dst) : "v"(voff), "s"(sbase), "n"(OFF) : "memory");
}
typedef short s16x12 __attribute__((ext_vector_type(12)));
DI s16x4 lds_tr4(unsigned a) { return __builtin_amdgcn_ds_read_tr16_b64_v4i16((LAS s16x4*)(uintptr_t)a); }
template <int CTRL> DI float dpp_add(float v) {
  return v + __builtin_bit_cast(float, __builtin_amdgcn_update_dpp(0, __builtin_bit_cast(int, v), CTRL, 0xf, 0xf, true));
}
DI float row16_sum(float v) {
  return dpp_add<0x121>(dpp_add<0x122>(dpp_add<0x124>(dpp_add<0x128>(v))));
}
DI void fft_step_a(f32x4 (&a)[3], const bf16x8 (&ta)[6], unsigned a0, unsigned a1, unsigned a2) {
  const s16x4 z0 = lds_tr4(a0), z1 = lds_tr4(a1), z2 = lds_tr4(a2);
  const bf16x8 lo = __builtin_bit_cast(bf16x8, __builtin_shufflevector(z0, z1, 0, 1, 2, 3, 4, 5, 6, 7));
  const bf16x8 hi = __builtin_bit_cast(bf16x8, __builtin_shufflevector(z1, z2, 0, 1, 2, 3, 4, 5, 6, 7));
#pragma unroll
  for (int T = 0; T < 3; ++T) a[T] = mfma16(ta[T], lo, f32x4{});
#pragma unroll
  for (int T = 0; T < 3; ++T) a[T] = mfma16(ta[3 + T], hi, a[T]);
}

template <int MODE>
DI void fft640_block(int blk, unsigned lds0, const u16* __restrict__ src, u16* __restrict__ dst, float* __restrict__ part TSREF) {
  const int lane = threadIdx.x & 63, w = uni(threadIdx.x >> 6), g = lane >> 4, i = lane & 15;
  const unsigned lane16 = lane * 16;
  if constexpr (MODE != 2) {
#pragma unroll
    for (int r = 0; r < 4; ++r) {
      const int rho = 4 * w + r;
      const int grow = MODE == 0 ? row_of(blk * 32 + rho) : blk * 32 + rho;
      glds16<0>((const char*)src + (size_t)grow * 1280, lane16, lds0 + rho * RP);
    }
    if (lane < 16) {
#pragma unroll
      for (int r = 0; r < 4; ++r) {
        const int rho = 4 * w + r;
        const int grow = MODE == 0 ? row_of(blk * 32 + rho) : blk * 32 + rho;
        glds16<1024>((const char*)src + (size_t)grow * 1280, lane16, lds0 + rho * RP);
      }
    }
  } else {
    const unsigned vo = (lane >> 2) * (FF_ * 2) + (lane & 3) * 16;
#pragma unroll
    for (int p = 0; p < 5; ++p)
      glds16<0>((const char*)src + blk * 64 + (size_t)(5 * w + p) * (16 * FF_ * 2), vo, lds0 + (5 * w + p) * 1088);
  }
  if constexpr (MODE == 3) {
    wait_bar<0>();
    const unsigned rb = lds0 + (4 * g + (i >> 2)) * RP + (i & 3) * 8 + w * 160;
#pragma unroll
    for (int q = 0; q < 5; ++q)
      wst((char*)dst + ((size_t)((5 * w + q) * K2S + blk) * 64 + lane) * 16, lds_tr(rb + q * 32, rb + q * 32 + 16 * RP));
    return;
  }
  const char* tb = (const char*)&g_FFT;
  constexpr int NC = MODE == 2 ? 10 : 5;
  bf16x8 ta[6], tc[NC];
  sfor<6>([&](auto uc) { constexpr int u = decltype(uc)::value; gld16<(u & 3) * 1024>(ta[u], tb + 40960 + (u >> 2) * 4096, lane16); });
  sfor<NC>([&](auto uc) {
    constexpr int u = decltype(uc)::value;
    gld16<(u & 3) * 1024>(tc[u], tb + (MODE == 2 ? (w >> 1) * 10240 : w * 5120) + (u >> 2) * 4096, lane16);
  });
  asm volatile("s_waitcnt vmcnt(%6)" : "+v"(ta[0]), "+v"(ta[1]), "+v"(ta[2]), "+v"(ta[3]), "+v"(ta[4]), "+v"(ta[5]) : "n"(NC) : "memory");
  if constexpr (MODE == 2) lgkm_bar();
  TS(1);
  if constexpr (MODE < 2) {
    f32x4 a[4][3];
#pragma unroll
    for (int r = 0; r < 4; ++r) {
      const unsigned rb = lds0 + (4 * w + r) * RP;
      fft_step_a(a[r], ta, rb + lane * 8, rb + 512 + lane * 8, rb + 1024 + (lane & 31) * 8);
    }
    const unsigned wb = lds0 + 4 * w * RP + g * 128 + i * 4;
#pragma unroll
    for (int r = 0; r < 4; ++r)
#pragma unroll
      for (int T = 0; T < 2; ++T) {
        *(LAS unsigned*)(uintptr_t)(wb + r * RP + T * 512) = pack2(a[r][T][0], a[r][T][1]);
        *(LAS unsigned*)(uintptr_t)(wb + r * RP + T * 512 + 64) = pack2(a[r][T][2], a[r][T][3]);
      }
    if (g < 2) {
#pragma unroll
      for (int r = 0; r < 4; ++r) {
        *(LAS unsigned*)(uintptr_t)(wb + r * RP + 1024) = pack2(a[r][2][0], a[r][2][1]);
        *(LAS unsigned*)(uintptr_t)(wb + r * RP + 1024 + 64) = pack2(a[r][2][2], a[r][2][3]);
      }
    }
  } else {
    const int j = w & 3, ft = w >> 2;
    const unsigned rb = lds0 + (4 * g + (i >> 2)) * 1088 + j * 256 + (i & 3) * 8 + ft * 32;
    const unsigned rt = lds0 + (32 + 4 * (g & 1) + (i >> 2)) * 1088 + j * 256 + (i & 3) * 8 + ft * 32;
    f32x4 a[4][3];
#pragma unroll
    for (int dd = 0; dd < 4; ++dd) fft_step_a(a[dd], ta, rb + dd * 64, rb + dd * 64 + 16 * 1088, rt + dd * 64);
    u32x4 u[3][2];
#pragma unroll
    for (int T = 0; T < 3; ++T)
#pragma unroll
      for (int dd = 0; dd < 4; ++dd) {
        u[T][0][dd] = pack2(a[dd][T][0], a[dd][T][1]);
        u[T][1][dd] = pack2(a[dd][T][2], a[dd][T][3]);
      }
    lgkm_bar();
    const unsigned wb = lds0 + (16 * ft + i) * RP + g * 128 + j * 16;
#pragma unroll
    for (int T = 0; T < 3; ++T)
      if (T < 2 || g < 2) {
        *(LAS u32x4*)(uintptr_t)(wb + T * 512) = u[T][0];
        *(LAS u32x4*)(uintptr_t)(wb + T * 512 + 64) = u[T][1];
      }
  }
  TS(2);
  lgkm_bar();
  asm volatile("s_waitcnt vmcnt(0)" : "+v"(tc[0]), "+v"(tc[1]), "+v"(tc[2]), "+v"(tc[3]), "+v"(tc[4])::"memory");
  TS(3);
  const unsigned db = lds0 + i * RP + g * 16;
  if constexpr (MODE == 2) {
    asm volatile("" : "+v"(tc[NC - 5]), "+v"(tc[NC - 4]), "+v"(tc[NC - 3]), "+v"(tc[NC - 2]), "+v"(tc[NC - 1]));
#pragma unroll
    for (int q = 0; q < 5; ++q) {
      const int G = 5 * (w >> 1) + q;
      const bf16x8 d = lds128(db + (w & 1) * 16 * RP + G * 64);
      f32x4 a = mfma16(tc[2 * q], d, f32x4{});
      const f32x4 b = mfma16(tc[2 * q + 1], d, f32x4{});
      if (q == 0 && w < 2 && g == 0) a[1] = 0.f;
      wst((char*)dst + ((size_t)((2 * blk + (w & 1)) * K1S + G) * 64 + lane) * 16, pack8(a, b));
    }
    return;
  }
  float sq[2] = {};
#pragma unroll
  for (int q = 0; q < 5; ++q) {
    const int u = 5 * w + q, G = u >> 1;
    const bf16x8 d0 = lds128(db + G * 64), d1 = lds128(db + 16 * RP + G * 64);
    if constexpr (MODE == 1) {
      const f32x4 a = mfma16(d0, tc[q], f32x4{}), b = mfma16(d1, tc[q], f32x4{});
      wst((char*)dst + ((size_t)(u * K2S + blk) * 64 + lane) * 16, pack8(a, b));
    } else {
      f32x4 a = mfma16(tc[q], d0, f32x4{}), b = mfma16(tc[q], d1, f32x4{});
      if constexpr (MODE == 0) {
        char* o = (char*)dst + (size_t)(2 * blk) * XP + u * 512 + lane * 8;
        wst(o, pack4(a));
        wst(o + XP, pack4(b));
      }
      if (q == 0 && w == 0 && g == 0) { a[1] = 0.f; b[1] = 0.f; }
      if constexpr (MODE == 0) {
        sq[0] += (a[0] * a[0] + a[1] * a[1]) + (a[2] * a[2] + a[3] * a[3]);
        sq[1] += (b[0] * b[0] + b[1] * b[1]) + (b[2] * b[2] + b[3] * b[3]);
      } else {
        char* o = (char*)dst + ((size_t)(2 * blk * K1S + G) * 64 + lane) * 16 + (u & 1) * 8;
        wst(o, pack4(a));
        wst(o + B1S, pack4(b));
      }
    }
  }
  if constexpr (MODE == 0) {
    float s = (g & 1) ? sq[1] : sq[0];
    s += __shfl_xor((g & 1) ? sq[0] : sq[1], 16);
    s += __shfl_xor(s, 32);
    if (g < 2) wst(part + w * S_ + blk * 32 + lane, s);
  }
}

DI void prefetch32k(const void* p, int blk) {
  u32x4 v[4];
#pragma unroll
  for (int q = 0; q < 4; ++q) gldv<0>(v[q], (const char*)p + (size_t)blk * 32768 + q * 8192, threadIdx.x * 16);
  asm volatile("s_waitcnt vmcnt(0)" : "+v"(v[0]), "+v"(v[1]), "+v"(v[2]), "+v"(v[3])::"memory");
}
#define K1_PF 120
constexpr int K1_X = 64, K1_WT = 160;
__global__ __launch_bounds__(512) void k1_prep(const u16* __restrict__ x, const u16* __restrict__ wr, const u16* __restrict__ wi,
                                              const u16* __restrict__ w1, const u16* __restrict__ w2, u16* __restrict__ X0T,
                                              float* __restrict__ part, unsigned* __restrict__ WT,
                                              const u16* __restrict__ out DBGARG) {
  TSDECL; TS(0);
  extern __shared__ __attribute__((aligned(16))) char smem[];
  const unsigned lds0 = (unsigned)(uintptr_t)(LAS char*)smem;
  const int tid = threadIdx.x;
  int b = blockIdx.x;
  if (b < K1_X) {
    fft640_block<0>(b, lds0, x, X0T, part TSPASS);
  } else if (b >= K1_X + K1_WT) {
    b -= K1_X + K1_WT;
    if (b < K1_PF + PFO) prefetch32k(b < 60 ? w1 : b < 120 ? w2 : out, b < 60 ? b : b < 120 ? b - 60 : b - 120);
    else prefetch32k(part + 8 * S_, b - (K1_PF + PFO));
  } else {
    b -= K1_X;
    if (b < 4) wst((unsigned*)(part + 16 * S_) + b * 512 + tid, 0u);
    const int kt = b % 10, ot = b / 10;
    const int kc = tid & 31, r = tid >> 5, e = r & 7;
    const int t0 = 16 * (ot >> 2) + (r >> 3) + 64 * (16 * (e >> 2) + 4 * (ot & 3) + (e & 3));
    const u16* pr = wr + t0 * 321 + kt * 32 + kc + 1;
    const u16* pi = wi + t0 * 321 + kt * 32 + kc + 1;
    unsigned v[8];
#pragma unroll
    for (int it = 0; it < 8; ++it) v[it] = (unsigned)pr[it * 642] | ((unsigned)pi[it * 642] << 16);
    const unsigned wa = lds0 + (kc * 132 + r) * 4;
#pragma unroll
    for (int it = 0; it < 8; ++it) *(LAS unsigned*)(uintptr_t)(wa + it * 64) = v[it];
    lgkm_bar();
#pragma unroll
    for (int it = 0; it < 2; ++it) {
      const int kc2 = r + 16 * it;
      const int c = (kt * 32 + kc2 + 1) % 320, rr = c % 40, j = c / 40;
      const int cp = (rr == 0 ? 0 : rr == 20 ? 8 : rr < 20 ? 16 * rr : 16 * (40 - rr) + 8) + j;
      wst(WT + cp * 2048 + ot * 128 + kc * 4, *(const LAS u32x4*)(uintptr_t)(lds0 + (kc2 * 132 + kc * 4) * 4));
    }
  }
  TSOUT;
}

struct Cx { float c, s; };
DI Cx cis(float rev) { return Cx{__builtin_amdgcn_cosf(rev), __builtin_amdgcn_sinf(rev)}; }
DI Cx cmul(Cx a, Cx b) { return Cx{a.c * b.c - a.s * b.s, a.c * b.s + a.s * b.c}; }
constexpr int C_CH = 576, C_PL = 8 * C_CH, C_OP = 32 * 272;
constexpr int C_MID = 2 * C_OP, C_HALF = C_MID + 2 * C_PL, C_LDS = 2 * C_HALF;
#define PDELAY 0
template <bool SECOND>
__global__ __launch_bounds__(512) void k2_cols(const u16* Xin, const float* __restrict__ part, const unsigned* __restrict__ WT,
                                              u16* Xout, const u16* __restrict__ wa, u16* __restrict__ Wa,
                                              const u16* __restrict__ wb, u16* __restrict__ Wb DBGARG) {
  TSDECL; TS(0);
  extern __shared__ __attribute__((aligned(16))) char smem[];
  if (blockIdx.x >= 160) {
    const unsigned l0 = (unsigned)(uintptr_t)(LAS char*)smem;
    const int b = blockIdx.x - 160;
    if (PDELAY) __builtin_amdgcn_s_sleep(PDELAY);
    if constexpr (SECOND) {
      fft640_block<3>(b, l0, wa, Wa, nullptr TSPASS);
    }
    else if (b < 48) fft640_block<2>(b, l0, wa, Wa, nullptr TSPASS);
    else fft640_block<1>(b - 48, l0, wb, Wb, nullptr TSPASS);
    TSOUT;
    return;
  }
  const int half = uni(threadIdx.x >> 8), c = 2 * blockIdx.x + half;
  const unsigned ldsb = (unsigned)(uintptr_t)(LAS char*)smem, lds0 = ldsb + half * C_HALF;
  const int tid = threadIdx.x & 255, lane = tid & 63, w = uni(tid >> 6), g = lane >> 4, i = lane & 15;
  const int T = threadIdx.x;
  f32x4 ps[8];
  u32x4 xin[2], wv[2], res[2];
  const char* xb = (const char*)Xin + blockIdx.x * 128;
  const unsigned vin = (T >> 2) * XP + (T & 3) * 32, vout = (T >> 3) * XP + (T & 7) * 16;
  gldv<0>(xin[0], xb, vin);
  gldv<16>(xin[1], xb, vin);
#pragma unroll
  for (int p = 0; p < 8; ++p) gldv<0>(ps[p], part + p * S_, T * 16);
  gldv<0>(wv[0], WT + (size_t)c * 2048, tid * 32);
  gldv<16>(wv[1], WT + (size_t)c * 2048, tid * 32);
  gldv<0>(res[0], xb, vout);
  gldv<0>(res[1], xb + 64 * XP, vout);
  __builtin_amdgcn_sched_barrier(0);
  bf16x8 f1r[2], f1i[2], f1n[2], f2r[2], f2i[2], f2n[2];
  const int k1c = 16 * w + i;
  const unsigned odd = (i & 1) ? 0x80008000u : 0u;
  {
    const Cx st = cis((float)k1c * (-1.f / 64.f));
    Cx v = cis((float)((g * k1c) & 7) * (-1.f / 8.f));
    v.c *= 0.125f; v.s *= 0.125f;
    f32x4 cr[2], ci[2];
#pragma unroll
    for (int j = 0; j < 8; ++j) {
      cr[j >> 2][j & 3] = v.c; ci[j >> 2][j & 3] = v.s;
      if (j < 7) v = cmul(v, st);
    }
    f1r[0] = pack8(cr[0], cr[1]);
    f1i[0] = pack8(ci[0], ci[1]);
    f1r[1] = __builtin_bit_cast(bf16x8, __builtin_bit_cast(u32x4, f1r[0]) ^ odd);
    f1i[1] = __builtin_bit_cast(bf16x8, __builtin_bit_cast(u32x4, f1i[0]) ^ odd);
    f1n[0] = neg8(f1i[0]);
    f1n[1] = neg8(f1i[1]);
  }
  {
    const Cx st = cis((float)i * (-1.f / 32.f));
    Cx v = cis((float)((g * i) & 7) * (-1.f / 8.f));
    v.c *= 0.17677669529663688f; v.s *= 0.17677669529663688f;
    f32x4 cr, ci;
#pragma unroll
    for (int j = 0; j < 4; ++j) {
      cr[j] = v.c; ci[j] = v.s;
      if (j < 3) v = cmul(v, st);
    }
    const u32x2 pr = pack4(cr), pi = pack4(ci);
    const u32x4 r0 = {pr[0], pr[1], pr[0] ^ odd, pr[1] ^ odd}, i0 = {pi[0], pi[1], pi[0] ^ odd, pi[1] ^ odd};
    f2r[0] = __builtin_bit_cast(bf16x8, r0);
    f2i[0] = __builtin_bit_cast(bf16x8, i0);
    f2r[1] = __builtin_bit_cast(bf16x8, r0 ^ 0x80000000u);
    f2i[1] = __builtin_bit_cast(bf16x8, i0 ^ 0x80000000u);
    f2n[0] = neg8(f2i[0]);
    f2n[1] = neg8(f2i[1]);
  }
  f32x4 twc[2], tws[2], tvc[2], tvs[2];
  {
    const Cx st = cis((float)k1c * (1.f / 2048.f)), jp = cis((float)k1c * (1.f / 128.f));
    Cx v = cis((float)(g * k1c) * (1.f / 512.f));
#pragma unroll
    for (int r = 0; r < 4; ++r) {
      const Cx u = cmul(v, jp);
      twc[0][r] = v.c; tws[0][r] = v.s; twc[1][r] = u.c; tws[1][r] = u.s;
      if (r < 3) v = cmul(v, st);
    }
#pragma unroll
    for (int mt = 0; mt < 2; ++mt) {
      const Cx s2 = cis((float)(16 * mt + i) * (1.f / 2048.f));
      Cx u = cis((float)((16 * mt + i) * (4 * w + g)) * (1.f / 512.f));
#pragma unroll
      for (int r = 0; r < 4; ++r) {
        tvc[mt][r] = u.c; tvs[mt][r] = u.s;
        if (r < 3) u = cmul(u, s2);
      }
    }
  }
  asm volatile("" : "+v"(f1r[0]), "+v"(f1r[1]), "+v"(f1i[0]), "+v"(f1i[1]), "+v"(f1n[0]), "+v"(f1n[1]), "+v"(f2r[0]), "+v"(f2r[1]),
               "+v"(f2i[0]), "+v"(f2i[1]), "+v"(f2n[0]), "+v"(f2n[1]));
  asm volatile("" : "+v"(twc[0]), "+v"(twc[1]), "+v"(tws[0]), "+v"(tws[1]), "+v"(tvc[0]), "+v"(tvc[1]), "+v"(tvs[0]), "+v"(tvs[1]));
  TS(1);
  asm volatile("s_waitcnt vmcnt(4)" : "+v"(ps[0]), "+v"(ps[1]), "+v"(ps[2]), "+v"(ps[3]), "+v"(ps[4]), "+v"(ps[5]), "+v"(ps[6]), "+v"(ps[7]),
               "+v"(xin[0]), "+v"(xin[1])::"memory");
  {
    f32x4 rs = ((ps[0] + ps[1]) + (ps[2] + ps[3])) + ((ps[4] + ps[5]) + (ps[6] + ps[7]));
#pragma unroll
    for (int e = 0; e < 4; ++e) rs[e] = rsqrtf(rs[e] * (1.f / D_) + EPS_);
    const unsigned ad = ldsb + ((T & 15) >> 1) * C_CH + (T >> 4) * 16 + (T & 1) * 8;
#pragma unroll
    for (int cc = 0; cc < 2; ++cc) {
      const unsigned x0 = xin[0][cc], x1 = xin[0][2 + cc], x2 = xin[1][cc], x3 = xin[1][2 + cc];
      const u32x2 re = {pack2(bflo(x0) * rs[0], bflo(x1) * rs[1]), pack2(bflo(x2) * rs[2], bflo(x3) * rs[3])};
      u32x2 im = {pack2(bfhi(x0) * rs[0], bfhi(x1) * rs[1]), pack2(bfhi(x2) * rs[2], bfhi(x3) * rs[3])};
      if (cc == 0 && blockIdx.x == 0) im = u32x2{0, 0};
      *(LAS u32x2*)(uintptr_t)(ad + cc * C_HALF) = re;
      *(LAS u32x2*)(uintptr_t)(ad + cc * C_HALF + C_PL) = im;
    }
  }
  asm volatile("s_waitcnt vmcnt(2) lgkmcnt(0)\n\ts_barrier" : "+v"(wv[0]), "+v"(wv[1])::"memory");
  TS(2);
  if constexpr (SECOND) {
    const int wv8 = uni(threadIdx.x >> 6), idx = blockIdx.x >> 3;
    const unsigned dump = ldsb + C_LDS + wv8 * 1024;
    if (idx * 8 + wv8 < 46) glds16<0>((const char*)&g_FFT + (46 + idx * 8 + wv8) * 1024, lane * 16, dump);
  }
  const unsigned rd = lds0 + g * C_CH + i * 16;
  {
    f32x4 ar[2] = {}, ai[2] = {};
#pragma unroll
    for (int mt = 0; mt < 2; ++mt)
#pragma unroll
      for (int ks = 0; ks < 2; ++ks) {
        const bf16x8 ur = lds128(rd + ks * 4 * C_CH + mt * 256), ui = lds128(rd + C_PL + ks * 4 * C_CH + mt * 256);
        ar[mt] = mfma16(ur, f1r[ks], ar[mt]);
        ai[mt] = mfma16(ur, f1i[ks], ai[mt]);
        ar[mt] = mfma16(ui, f1n[ks], ar[mt]);
        ai[mt] = mfma16(ui, f1r[ks], ai[mt]);
      }
    f32x4 br[2], bi[2];
#pragma unroll
    for (int mt = 0; mt < 2; ++mt) {
      br[mt] = ar[mt] * twc[mt] + ai[mt] * tws[mt];
      bi[mt] = ai[mt] * twc[mt] - ar[mt] * tws[mt];
    }
    bf16x8 pr = pack8(br[0], br[1]), pi = pack8(bi[0], bi[1]);
    f32x4 yr[2] = {}, yi[2] = {};
#pragma unroll
    for (int mt = 0; mt < 2; ++mt) {
      yr[mt] = mfma16(f2r[mt], pr, yr[mt]);
      yi[mt] = mfma16(f2i[mt], pr, yi[mt]);
      yr[mt] = mfma16(f2n[mt], pi, yr[mt]);
      yi[mt] = mfma16(f2r[mt], pi, yi[mt]);
    }
#pragma unroll
    for (int mt = 0; mt < 2; ++mt) {
      const u32x4 ww = wv[mt];
#pragma unroll
      for (int r = 0; r < 4; ++r) {
        const float wr_ = bflo(ww[r]), wi_ = bfhi(ww[r]);
        br[mt][r] = yr[mt][r] * wr_ - yi[mt][r] * wi_;
        bi[mt][r] = yr[mt][r] * wi_ + yi[mt][r] * wr_;
      }
    }
    pr = pack8(br[0], br[1]);
    pi = pack8(bi[0], bi[1]);
    f32x4 zr[2] = {}, zi[2] = {};
#pragma unroll
    for (int nt = 0; nt < 2; ++nt) {
      zr[nt] = mfma16(pr, f2r[nt], zr[nt]);
      zi[nt] = mfma16(pi, f2r[nt], zi[nt]);
      zr[nt] = mfma16(pi, f2i[nt], zr[nt]);
      zi[nt] = mfma16(pr, f2n[nt], zi[nt]);
    }
    const unsigned ad = lds0 + C_MID + (2 * w + (g >> 1)) * C_CH + i * 16 + (g & 1) * 8;
#pragma unroll
    for (int nt = 0; nt < 2; ++nt) {
      const f32x4 a = zr[nt] * tvc[nt] - zi[nt] * tvs[nt];
      const f32x4 b = zi[nt] * tvc[nt] + zr[nt] * tvs[nt];
      *(LAS u32x2*)(uintptr_t)(ad + nt * 256) = pack4(a);
      *(LAS u32x2*)(uintptr_t)(ad + C_PL + nt * 256) = pack4(b);
    }
  }
  lgkm_bar();
  TS(3);
  {
    const unsigned r2 = rd + C_MID;
    const unsigned ad = lds0 + i * 272 + w * 64 + g * 16;
#pragma unroll
    for (int nt = 0; nt < 2; ++nt) {
      f32x4 vr = {}, vi = {};
#pragma unroll
      for (int ks = 0; ks < 2; ++ks) {
        const bf16x8 qr = lds128(r2 + ks * 4 * C_CH + nt * 256), qi = lds128(r2 + C_PL + ks * 4 * C_CH + nt * 256);
        vr = mfma16(f1r[ks], qr, vr);
        vi = mfma16(f1r[ks], qi, vi);
        vr = mfma16(f1i[ks], qi, vr);
        vi = mfma16(f1n[ks], qr, vi);
      }
      *(LAS f32x4*)(uintptr_t)(ad + nt * 16 * 272) = vr;
      *(LAS f32x4*)(uintptr_t)(ad + C_OP + nt * 16 * 272) = vi;
    }
  }
  asm volatile("s_waitcnt vmcnt(0) lgkmcnt(0)\n\ts_barrier" : "+v"(res[0]), "+v"(res[1])::"memory");
  TS(4);
  const unsigned ro = ldsb + (T >> 5) * 272 + (T & 31) * 8;
#pragma unroll
  for (int hh = 0; hh < 2; ++hh) {
    f32x2 v[4];
#pragma unroll
    for (int p = 0; p < 4; ++p) v[p] = *(const LAS f32x2*)(uintptr_t)(ro + hh * 16 * 272 + (p >> 1) * C_HALF + (p & 1) * C_OP);
    if (blockIdx.x == 0) v[1] = f32x2{0.f, 0.f};
    const u32x4 r = res[hh];
    const u32x4 o = {pack2(bflo(r[0]) + v[0][0], bfhi(r[0]) + v[1][0]), pack2(bflo(r[1]) + v[2][0], bfhi(r[1]) + v[3][0]),
                     pack2(bflo(r[2]) + v[0][1], bfhi(r[2]) + v[1][1]), pack2(bflo(r[3]) + v[2][1], bfhi(r[3]) + v[3][1])};
    wst((char*)Xout + blockIdx.x * 128 + hh * 64 * XP + vout, o);
  }
  TSOUT;
}

DI bf16x8 lds_2x64(unsigned a) {
  const u32x2 lo = *(const LAS u32x2*)(uintptr_t)a, hi = *(const LAS u32x2*)(uintptr_t)(a + 512);
  return __builtin_bit_cast(bf16x8, u32x4{lo[0], lo[1], hi[0], hi[1]});
}
constexpr int G1_EX = 81920, G1_ST = G1_EX + 64 * 1024, G1_CN = G1_ST + 512, G1_IF = G1_CN + 64, IFP = 656, G1_LDS = G1_IF + 16 * IFP;
template <bool ARRIVE> DI void g1_sync(unsigned ca, int lane) {
  if (ARRIVE && lane == 0) __hip_atomic_fetch_add((LAS unsigned*)(uintptr_t)ca, 1u, __ATOMIC_RELAXED, __HIP_MEMORY_SCOPE_WORKGROUP);
  while (uni(*(volatile LAS unsigned*)(uintptr_t)ca) < 4) {}
  asm volatile("" ::: "memory");
}
template <int KM, int K0, int NS, int XH, int XI, int D, int A, int NX, class F>
DI void g1_body(f32x4 (&acc)[3][4], f32x4& dg, unsigned lds0, int lane, int w, const char* xb, const char* wb, const char* wb2,
                F&& extra TSREF) {
  constexpr int XBAR = XI + D;
  static_assert(XBAR < XH && XI < A, "");
  constexpr auto issued = [](int s) { return s >= A || s + 1 + D > NS ? NS : s + 1 + D; };
  const unsigned lane16 = lane * 16, xd = lds0 + (w * 20 + K0) * 1024;
  const int cx = w ^ (2 * KM);
  auto xissue = [&](auto sc) {
    constexpr int s = decltype(sc)::value;
    glds16<(s & 3) * 1024>(xb + K0 * 1024 + (s >> 2) * 4096, lane16, xd + (s >> 2) * 4096);
  };
  sfor<XH>(xissue);
  if (KM == 0 && threadIdx.x < 8) *(LAS unsigned*)(uintptr_t)(lds0 + G1_CN + threadIdx.x * 4) = 0;
  lgkm_bar();
  auto gsync = [&](int n) { g1_sync<true>(lds0 + G1_CN + (KM * 2 + n) * 4, lane); };
  bf16x8 wq[NS][3], xf[2][4];
  auto wissue = [&](auto sc) {
    constexpr int s = decltype(sc)::value;
    gld16<(s & 3) * 1024>(wq[s][0], wb + K0 * 1024 + (s >> 2) * 4096, lane16);
    gld16<(s & 3) * 1024>(wq[s][1], wb + B1S + K0 * 1024 + (s >> 2) * 4096, lane16);
    gld16<(s & 3) * 1024>(wq[s][2], wb2 + K0 * 1024 + (s >> 2) * 4096, lane16);
  };
  sfor<D>(wissue);
  const unsigned dcm = lane < 16 ? 0xffffu : 0xffffffffu;
  unsigned xa[4];
#pragma unroll
  for (int c = 0; c < 4; ++c) xa[c] = lds0 + ((c ^ cx) * 20 + K0) * 1024 + lane * 8;
  auto rd = [&](auto sc) {
    constexpr int s = decltype(sc)::value;
#pragma unroll
    for (int c = 0; c < 4; ++c) xf[s & 1][c] = lds_2x64(xa[c] + s * 1024);
  };
  TS(1);
  asm volatile("s_waitcnt vmcnt(%0)" ::"n"(3 * D) : "memory");
  gsync(0);
  TS(2);
  rd(std::integral_constant<int, 0>{});
  sfor<NS>([&](auto sc) {
    constexpr int s = decltype(sc)::value;
    constexpr int have = issued(s - 1), next = issued(s);
    constexpr int pend = 3 * (have - 1 - s) + (s > XI && s < XBAR ? NS - XH : 0) + (s > A ? NX : 0);
    if constexpr (s + 1 < NS && s != XBAR) rd(std::integral_constant<int, s + 1>{});
    __builtin_amdgcn_sched_barrier(0);
    qwaitv<pend, 3>(wq[s]);
    if constexpr (s == XBAR) {
      gsync(1);
      rd(std::integral_constant<int, s + 1>{});
      __builtin_amdgcn_sched_barrier(0);
    }
    TS(8 + s);
#pragma unroll
    for (int q = 0; q < 3; ++q)
#pragma unroll
      for (int c = 0; c < 4; ++c) acc[q][c] = mfma16(wq[s][q], xf[s & 1][c], acc[q][c]);
    u32x4 u = __builtin_bit_cast(u32x4, xf[s & 1][0]);
    if constexpr (K0 + s == 0) u[0] &= dcm;
    const bf16x8 v = __builtin_bit_cast(bf16x8, u);
    dg = mfma16(v, v, dg);
    __builtin_amdgcn_sched_barrier(0);
    if constexpr (s == XI) sfor<NS - XH>([&](auto kc) { xissue(std::integral_constant<int, XH + decltype(kc)::value>{}); });
    sfor<next - have>([&](auto kc) { wissue(std::integral_constant<int, have + decltype(kc)::value>{}); });
    if constexpr (s == A) extra();
    __builtin_amdgcn_sched_barrier(0);
  });
}
#define G1P0 0, 10, 5, 1, 3, 5
#define G1P1 10, 10, 5, 1, 3, 5
DI f32x4 silu4(f32x4 v) {
  f32x4 o;
#pragma unroll
  for (int r = 0; r < 4; ++r) o[r] = v[r] * __builtin_amdgcn_rcpf(1.f + __builtin_amdgcn_exp2f(v[r] * -1.4426950408889634f));
  return o;
}
template <int OFF, class T> DI void gld_sc1(T& dst, const void* sbase, unsigned voff) {
  if constexpr (sizeof(T) == 16)
    asm volatile("global_load_dwordx4 %0, %1, %2 offset:%3 sc1" : "=v"(dst) : "v"(voff), "s"(sbase), "n"(OFF) : "memory");
  else if constexpr (sizeof(T) == 8)
    asm volatile("global_load_dwordx2 %0, %1, %2 offset:%3 sc1" : "=v"(dst) : "v"(voff), "s"(sbase), "n"(OFF) : "memory");
  else
    asm volatile("global_load_dword %0, %1, %2 offset:%3 sc1" : "=v"(dst) : "v"(voff), "s"(sbase), "n"(OFF) : "memory");
}
constexpr int G2_GO = G1_CN + 32;
#define XMAP 0
#define W2A 2
template <bool LAST, int km>
DI void mlp_body(const u16* __restrict__ XT, const u16* __restrict__ W1F, u16* H, const u16* __restrict__ W2, u16* R, u16* __restrict__ Y,
                 float* __restrict__ part, unsigned* flags DBGARG) {
  TSDECL; TS(0);
  extern __shared__ __attribute__((aligned(16))) char smem[];
  const unsigned lds0 = (unsigned)(uintptr_t)(LAS char*)smem;
  const int tid = threadIdx.x, lane = tid & 63, w = uni(tid >> 6) & 3, wave = w + 4 * km, g = lane >> 4, i = lane & 15;
  const int b = blockIdx.x, xcd = b & 7, j = b >> 3;
  const int mt = (xcd >> 1) * 8 + (j & 7), nt = (xcd & 1) * 4 + (j >> 3);
  const int e = w & 1, hf = w >> 1, cx = w ^ (2 * km);
  const unsigned lane16 = lane * 16;
  unsigned* fl = flags + mt * 32;
  const char* xb = (const char*)XT + (size_t)(mt * 4 + w) * XP;
  const char* a2 = (const char*)W2 + (size_t)nt * 5 * B2S + wave * 6144;
  bf16x8 wr[4][5];
  for (int ft = nt;;) {
    if (km == 0 && tid == 0) wst(fl + ft, 1u);
    const char* wb = (const char*)W1F + (size_t)(ft * 12 + hf * 6 + 2 * e) * B1S;
    f32x4 acc[3][4] = {};
    f32x4 dg = {};
    bf16x8 tc[5], ta[4];
    auto tabs = [&]() {
      if constexpr (LAST) {
        const char* tb = (const char*)&g_FFT + 46 * 1024;
        sfor<5>([&](auto uc) { constexpr int u = decltype(uc)::value; gld16<(u & 3) * 1024>(tc[u], tb + ((ft & 1) * 20 + w * 5 + (u & 4)) * 1024, lane16); });
        gld16<0>(ta[0], tb + (40 + 2 * hf) * 1024, lane16);
        gld16<1024>(ta[1], tb + (40 + 2 * hf) * 1024, lane16);
        gld16<0>(ta[2], tb + 44 * 1024, lane16);
        gld16<1024>(ta[3], tb + 44 * 1024, lane16);
      }
    };
    if constexpr (km == 0) {
      g1_body<0, G1P0, LAST ? 9 : 0>(acc, dg, lds0, lane, w, xb, wb, wb + (4 - e) * B1S, tabs TSPASS);
      if constexpr (LAST)
        asm volatile("s_waitcnt vmcnt(0)" : "+v"(tc[0]), "+v"(tc[1]), "+v"(tc[2]), "+v"(tc[3]), "+v"(tc[4]), "+v"(ta[0]), "+v"(ta[1]),
                     "+v"(ta[2]), "+v"(ta[3])::"memory");
    } else g1_body<1, G1P1, 0>(acc, dg, lds0, lane, w, xb, wb, wb + (4 - e) * B1S, [] {} TSPASS);
    TS(3);
    auto w2issue = [&](auto sc) {
      constexpr int s = decltype(sc)::value;
#pragma unroll
      for (int q = 0; q < 5; ++q) gld16<s * 1024>(wr[s][q], a2 + q * B2S, lane16);
    };
    if constexpr (km == 0) sfor<4>(w2issue);
    {
      const unsigned ex = lds0 + G1_EX + wave * 8192 + lane16;
#pragma unroll
      for (int c = 2; c < 4; ++c)
#pragma unroll
        for (int q = 0; q < 2; ++q) *(LAS f32x4*)(uintptr_t)(ex + ((c - 2) * 2 + q) * 1024) = acc[q][c];
#pragma unroll
      for (int c = 1; c < 4; ++c) *(LAS f32x4*)(uintptr_t)(ex + (4 + c) * 1024) = acc[2][c];
      const int q4 = i >> 2, p4 = i & 3;
      if (g == q4) {
        const float d0 = dg[0], d1 = dg[1], d2 = dg[2], d3 = dg[3];
        *(LAS float*)(uintptr_t)(lds0 + G1_ST + km * 256 + (cx * 16 + i) * 4) = p4 == 0 ? d0 : p4 == 1 ? d1 : p4 == 2 ? d2 : d3;
      }
    }
    if constexpr (LAST) {
      if constexpr (km == 0) {
        g1_sync<false>(lds0 + G1_CN + 12, lane);
        const unsigned xs = lds0 + ((ft >> 1) * 20 + w * 5) * 1024 + lane * 8, mw = lds0 + G1_IF + i * IFP + g * 160 + w * 20;
#pragma unroll
        for (int q = 0; q < 5; ++q) {
          const f32x4 d = mfma16(tc[q], lds_2x64(xs + q * 1024), f32x4{});
          *(LAS unsigned*)(uintptr_t)(mw + q * 4) = pack2(d[0], d[1]);
          *(LAS unsigned*)(uintptr_t)(mw + q * 4 + 80) = pack2(d[2], d[3]);
        }
        asm volatile("s_waitcnt lgkmcnt(0)" ::: "memory");
        g1_sync<true>(lds0 + G1_CN + 16, lane);
        const unsigned mr = lds0 + G1_IF + i * IFP + e * 320 + g * 16;
        bf16x8 b0[4], b1[4];
#pragma unroll
        for (int dd = 0; dd < 4; ++dd) { b0[dd] = lds128(mr + dd * 80); b1[dd] = lds128(mr + dd * 80 + 16); }
        char* yo = (char*)R + (size_t)(mt * 4 + (ft >> 1)) * XP + (16 * g + 2 * (ft & 1) + e) * 128 + i * 8;
        auto unit = [&](bf16x8 t0, bf16x8 t1, int T, bool st) {
          f32x4 o[4];
#pragma unroll
          for (int dd = 0; dd < 4; ++dd) o[dd] = mfma16(t1, b1[dd], mfma16(t0, b0[dd], f32x4{}));
          if (st) {
#pragma unroll
            for (int r = 0; r < 4; ++r) wst(yo + T * 8192 + r * 512, pack4(f32x4{o[0][r], o[1][r], o[2][r], o[3][r]}));
          }
        };
        unit(ta[0], ta[1], hf, true);
        if (hf == 0) unit(ta[2], ta[3], 2, g < 2);
      }
    }
    lgkm_bar();
    TS(4);
    if constexpr (km != 0) sfor<W2A>(w2issue);
    const unsigned ep = lds0 + G1_EX + (wave ^ 4) * 8192 + lane16, en = lds0 + G1_EX + (wave ^ 1) * 8192 + lane16,
                   eo = lds0 + G1_EX + (wave ^ 5) * 8192 + lane16;
    float rstd[2];
#pragma unroll
    for (int c = 0; c < 2; ++c) {
      const unsigned sa = lds0 + G1_ST + ((c ^ cx) * 16 + i) * 4;
      rstd[c] = rsqrtf((*(const LAS float*)(uintptr_t)sa + *(const LAS float*)(uintptr_t)(sa + 256)) * (1.f / D_) + EPS_);
    }
    char* hb = (char*)H + (size_t)(mt * 4 * K2S + ft * 6 + hf * 3) * 1024 + lane16;
#pragma unroll
    for (int c = 0; c < 2; ++c) {
      const u32x2 h0 = pack4(silu4((acc[0][c] + *(const LAS f32x4*)(uintptr_t)(ep + (c * 2) * 1024)) * rstd[c]));
      const u32x2 h1 = pack4(silu4((acc[1][c] + *(const LAS f32x4*)(uintptr_t)(ep + (c * 2 + 1) * 1024)) * rstd[c]));
      wst(hb + ((c ^ cx) * K2S + e) * 1024, u32x4{h0[0], h0[1], h1[0], h1[1]});
    }
    {
      const u32x2 h0 = pack4(silu4((acc[2][0] + *(const LAS f32x4*)(uintptr_t)(ep + 6 * 1024)) * rstd[0]));
      const u32x2 h1 = pack4(silu4((*(const LAS f32x4*)(uintptr_t)(en + 5 * 1024) + *(const LAS f32x4*)(uintptr_t)(eo + 7 * 1024)) * rstd[0]));
      wst(hb + (cx * K2S + 2) * 1024, e ? u32x4{h1[0], h1[1], h0[0], h0[1]} : u32x4{h0[0], h0[1], h1[0], h1[1]});
    }
#pragma unroll
    for (int s = 0; s < (km ? W2A : 4); ++s)
      asm volatile("s_waitcnt vmcnt(0)" : "+v"(wr[s][0]), "+v"(wr[s][1]), "+v"(wr[s][2]), "+v"(wr[s][3]), "+v"(wr[s][4])::"memory");
    asm volatile("s_waitcnt vmcnt(0)" ::: "memory");
    lgkm_bar();
    TS(5);
    if (km == 0 && tid == 0) wst(fl + 8 + ft, 1u);
    if constexpr (km != 0) sfor<4 - W2A>([&](auto sc) { w2issue(std::integral_constant<int, W2A + decltype(sc)::value>{}); });
#pragma unroll
    for (int q = 0; q < 5; ++q) {
      glds16<0>(a2 + q * B2S + 4096, lane16, lds0 + wave * 10240 + q * 2048);
      glds16<1024>(a2 + q * B2S + 4096, lane16, lds0 + wave * 10240 + q * 2048);
    }
    if (km == 0 && wave == 0) {
      int go = -1;
      for (int it = 0; it < (1 << 16); ++it) {
        unsigned v;
        gld_sc1<0>(v, fl, i * 4);
        asm volatile("s_waitcnt vmcnt(0)" : "+v"(v)::"memory");
        const unsigned m = (unsigned)__builtin_amdgcn_ballot_w64(v != 0) & 0xffffu;
        if ((m >> 8) == 0xffu) break;
        if ((m & 0xffu) != 0xffu) { go = __builtin_ctz(~m); break; }
      }
      if (lane == 0) *(LAS int*)(uintptr_t)(lds0 + G2_GO) = go;
    }
    lgkm_bar();
    const int go = uni(*(volatile LAS int*)(uintptr_t)(lds0 + G2_GO));
    if (go < 0) break;
    asm volatile("s_waitcnt vmcnt(0)" ::: "memory");
    lgkm_bar();
    ft = go;
  }
  TS(6);
  const int c2 = w;
  const char* b2 = (const char*)H + (size_t)mt * 4 * B2S + wave * 6144;
  f32x4 acc[5][4] = {};
  bf16x8 bf[4][4], af[5];
  u32x2 res[5];
  auto hissue = [&](auto sc) {
    constexpr int s = decltype(sc)::value;
#pragma unroll
    for (int c = 0; c < 4; ++c) gld_sc1<(s & 3) * 1024>(bf[s & 3][c], b2 + (size_t)(c ^ c2) * B2S + (s >> 2) * 4096, lane16);
  };
  sfor<4>(hissue);
  sfor<6>([&](auto sc) {
    constexpr int s = decltype(sc)::value;
    constexpr int pend = s <= 2 ? 12 : s == 3 ? 13 : s == 4 ? 9 : 5;
    bf16x8(&bb)[4] = bf[s & 3];
    asm volatile("s_waitcnt vmcnt(%4)" : "+v"(bb[0]), "+v"(bb[1]), "+v"(bb[2]), "+v"(bb[3]) : "n"(pend) : "memory");
    if constexpr (s < 4) asm volatile("" : "+v"(wr[s][0]), "+v"(wr[s][1]), "+v"(wr[s][2]), "+v"(wr[s][3]), "+v"(wr[s][4]));
    TS(20 + s);
    if constexpr (s >= 4) {
#pragma unroll
      for (int q = 0; q < 5; ++q) af[q] = lds128(lds0 + wave * 10240 + q * 2048 + (s - 4) * 1024 + lane16);
    }
#pragma unroll
    for (int q = 0; q < 5; ++q)
#pragma unroll
      for (int c = 0; c < 4; ++c) acc[q][c] = mfma16(s < 4 ? wr[s & 3][q] : af[q], bb[c], acc[q][c]);
    __builtin_amdgcn_sched_barrier(0);
    if constexpr (s < 2) hissue(std::integral_constant<int, s + 4>{});
    if constexpr (s == 2) {
#pragma unroll
      for (int q = 0; q < 5; ++q) gld_sc1<0>(res[q], (const char*)R + (size_t)(mt * 4 + w) * XP + (nt * 5 + q) * 512, lane * 8);
    }
    __builtin_amdgcn_sched_barrier(0);
  });
  TS(30);
  auto ulo = [&](int v) { return lds0 + v * 10240 + lane16; };
  auto uhi = [&](int v) { return lds0 + 81920 - 10240 + (v < 4 ? v * 10240 : v * 5120 + 20480) + lane16; };
  asm volatile("s_waitcnt lgkmcnt(0)" ::: "memory");
#pragma unroll
  for (int c = 1; c < 4; ++c)
#pragma unroll
    for (int q = 0; q < 5; ++q) *(LAS f32x4*)(uintptr_t)((c < 3 ? ulo(wave) : uhi(wave)) + ((c - 1) * 5 + q) * 1024) = acc[q][c];
  auto gather = [&](int q) {
    f32x4 u[3];
#pragma unroll
    for (int c = 1; c < 4; ++c) u[c - 1] = *(const LAS f32x4*)(uintptr_t)((c < 3 ? ulo(c ^ wave) : uhi(c ^ wave)) + ((c - 1) * 5 + q) * 1024);
    return (acc[q][0] + u[0]) + (u[1] + u[2]);
  };
  if constexpr (km == 0) {
    asm volatile("s_waitcnt lgkmcnt(0)" ::: "memory");
    g1_sync<true>(lds0 + G1_CN + 20, lane);
#pragma unroll
    for (int q = 0; q < 5; ++q) *(LAS f32x4*)(uintptr_t)(uhi(w) + (15 + q) * 1024) = gather(q);
    asm volatile("s_waitcnt vmcnt(0)" ::"v"(res[0]), "v"(res[1]), "v"(res[2]), "v"(res[3]), "v"(res[4]) : "memory");
    lgkm_bar();
    TSOUT;
    return;
  } else {
  f32x4 v[5];
  lgkm_bar();
  asm volatile("s_waitcnt vmcnt(0)" : "+v"(res[0]), "+v"(res[1]), "+v"(res[2]), "+v"(res[3]), "+v"(res[4])::"memory");
#pragma unroll
  for (int q = 0; q < 5; ++q) v[q] = gather(q) + (*(const LAS f32x4*)(uintptr_t)(uhi(w) + (15 + q) * 1024) + unpack4(res[q]));
  TS(31);
  if constexpr (!LAST) {
    float sq = 0.f;
    char* o = (char*)Y + (size_t)(mt * 4 + w) * XP + nt * 2560 + lane * 8;
#pragma unroll
    for (int q = 0; q < 5; ++q) {
      f32x4 u = v[q];
      wst(o + q * 512, pack4(u));
      if (q == 0 && nt == 0 && g == 0) u[1] = 0.f;
      sq += (u[0] * u[0] + u[1] * u[1]) + (u[2] * u[2] + u[3] * u[3]);
    }
    sq += __shfl_xor(sq, 16);
    sq += __shfl_xor(sq, 32);
    if (g == 0) wst(part + nt * S_ + mt * 64 + w * 16 + i, sq);
  } else {
    u16* o = Y + (size_t)row_of(mt * 64 + w * 16 + i) * D_ + nt * 80;
#pragma unroll
    for (int q = 0; q < 4; q += 2) {
      const u32x2 p0 = pack4(v[q]), p1 = pack4(v[q + 1]);
      const auto s0 = __builtin_amdgcn_permlane16_swap(p0[0], p1[0], false, false);
      const auto s1 = __builtin_amdgcn_permlane16_swap(p0[1], p1[1], false, false);
      wst(o + (q + (g & 1)) * 16 + (g >> 1) * 8, u32x4{s0[0], s1[0], s0[1], s1[1]});
    }
    wst(o + 64 + g * 4, pack4(v[4]));
  }
  TSOUT;
  }
}
template <bool LAST>
__global__ __launch_bounds__(512) void k3_mlp(const u16* __restrict__ XT, const u16* __restrict__ W1F, u16* H, const u16* __restrict__ W2,
                                             u16* R, u16* __restrict__ Y, float* __restrict__ part, unsigned* flags DBGARG) {
  if (uni(threadIdx.x) < 256) mlp_body<LAST, 0>(XT, W1F, H, W2, R, Y, part, flags DBGFWD);
  else mlp_body<LAST, 1>(XT, W1F, H, W2, R, Y, part, flags DBGFWD);
}

extern "C" void kernel_launch(void* const* d_in, const int* in_sizes, int n_in, void* d_out, int out_size, void* d_ws, size_t ws_size,
                              hipStream_t stream) {
  const u16* x = (const u16*)d_in[0];
  const u16* wr = (const u16*)d_in[1];
  const u16* wi = (const u16*)d_in[2];
  const u16* w1 = (const u16*)d_in[4];
  const u16* w2 = (const u16*)d_in[5];
  char* ws = (char*)d_ws;
  constexpr size_t XB = (size_t)XP * 128;
  u16* X0T = (u16*)(ws + 0 * XB);
  u16* X1T = (u16*)(ws + 1 * XB);
  u16* X2T = (u16*)(ws + 2 * XB);
  u16* X3T = (u16*)(ws + 3 * XB);
  u16* Y3 = (u16*)(ws + 4 * XB);
  unsigned* WT = (unsigned*)(ws + 5 * XB);
  u16* W1F = (u16*)(ws + 6 * XB);
  u16* W2F = (u16*)(ws + 7 * XB);
  u16* W2P = (u16*)(ws + 8 * XB);
  u16* H = (u16*)(ws + 9 * XB);
  u16* H2 = (u16*)(ws + 13 * XB);
  float* part0 = (float*)(ws + 12 * XB);
  float* part = part0 + 8 * S_;

  unsigned* flags = (unsigned*)(part + 8 * S_);
  (void)hipFuncSetAttribute((const void*)k3_mlp<false>, hipFuncAttributeMaxDynamicSharedMemorySize, G1_LDS);
  (void)hipFuncSetAttribute((const void*)k3_mlp<true>, hipFuncAttributeMaxDynamicSharedMemorySize, G1_LDS);

  k1_prep<<<K1_X + K1_WT + K1_PF + PFO + 2, 512, 32 * RP, stream>>>(x, wr, wi, w1, w2, X0T, part0, WT, (const u16*)d_out DBGPASS(0));
  k2_cols<false><<<160 + 96, 512, C_LDS, stream>>>(X0T, part0, WT, X1T, w1, W1F, w2, W2F DBGPASS(1));
  k3_mlp<false><<<256, 512, G1_LDS, stream>>>(X1T, W1F, H, W2F, X1T, X2T, part, flags DBGPASS(2));
  k2_cols<true><<<160 + 48, 512, C_LDS + 8192, stream>>>(X2T, part, WT, X3T, w2, W2P, w2, W2P DBGPASS(3));
  k3_mlp<true><<<256, 512, G1_LDS, stream>>>(X3T, W1F, H2, W2P, Y3, (u16*)d_out, part, flags + 1024 DBGPASS(4));
}
```
